```python
import torch

def _softmax(x, axis=-1):
    return torch.softmax(x, dim=axis)

def _gelu(x):
    return torch.nn.functional.gelu(x, approximate='tanh')

def _silu(x):
    return torch.nn.functional.silu(x)

def _sigmoid(x):
    return torch.sigmoid(x)

def _layernorm(x, axis=-1, eps=1e-05):
    m = x.mean(dim=axis, keepdim=True)
    v = x.var(dim=axis, keepdim=True, unbiased=False)
    return (x - m) / torch.sqrt(v + eps)

def _scatter_add(tgt, idx, src, axis=0):
    return tgt.clone().index_add_(axis, idx, src)

def _segment_sum(src, seg, n):
    out = torch.zeros((n,) + src.shape[1:], dtype=src.dtype)
    return out.index_add_(0, seg, src)

def _pad(x, pw):
    flat = tuple((v for pair in reversed(pw) for v in pair))
    return torch.nn.functional.pad(x, flat)
kernel_name = 'interleave_layers__memory_aug_recurrent_mem_tr__geglu_ffn__prompt'

def setup_inputs():

    _w = lambda a: (lambda t: t.to(torch.bfloat16) if t.is_floating_point() else t)(torch.as_tensor(a))

    def _A_setup():
        B, S, Dm = (1, 2048, 512)
        H, Dh = (32, 128)
        M, seg = (8, 32)
        seg = min(seg, S)
        S = S // seg * seg
        return {'x': _w((lambda *s: torch.randn(*s, dtype=torch.bfloat16) * 0.05)(B, S, Dm)), 'mem0': _w((lambda *s: torch.randn(*s, dtype=torch.bfloat16) * 0.8)(B, M, Dm)), 'Wq': _w((lambda *s: torch.randn(*s, dtype=torch.bfloat16) * 0.8)(Dm, H, Dh) * Dm ** (-0.5)), 'Wk': _w((lambda *s: torch.randn(*s, dtype=torch.bfloat16) * 0.8)(Dm, H, Dh) * Dm ** (-0.5)), 'Wv': _w((lambda *s: torch.randn(*s, dtype=torch.bfloat16) * 0.8)(Dm, H, Dh) * Dm ** (-0.5)), 'Wo': _w((lambda *s: torch.randn(*s, dtype=torch.bfloat16) * 0.8)(H, Dh, Dm) * (H * Dh) ** (-0.5)), 'seg_len': seg}
    _A_in = _A_setup()

    def _B_setup():
        B, S, D, F = (1, 2048, 512, 1792)
        return {'x': _w((lambda *s: torch.randn(*s, dtype=torch.bfloat16) * 0.05)(B, S, D)), 'w_gate': _w((lambda *s: torch.randn(*s, dtype=torch.bfloat16) * 0.05)(D, F)), 'w_up': _w((lambda *s: torch.randn(*s, dtype=torch.bfloat16) * 0.05)(D, F)), 'w_down': _w((lambda *s: torch.randn(*s, dtype=torch.bfloat16) * 0.05)(F, D))}
    _B_in = _B_setup()
    return {'A_x': _A_in['x'], 'A_mem0': _A_in['mem0'], 'A_Wq': _A_in['Wq'], 'A_Wk': _A_in['Wk'], 'A_Wv': _A_in['Wv'], 'A_Wo': _A_in['Wo'], 'A_seg_len': _A_in['seg_len'], 'B_x': _B_in['x'], 'B_w_gate': _B_in['w_gate'], 'B_w_up': _B_in['w_up'], 'B_w_down': _B_in['w_down']}

def reference(A_x, A_mem0, A_Wq, A_Wk, A_Wv, A_Wo, A_seg_len, B_x, B_w_gate, B_w_up, B_w_down):

    def _A_ref(x, mem0, Wq, Wk, Wv, Wo, seg_len):
        B, S, Dm = x.shape
        H, Dh = (Wq.shape[1], Wq.shape[2])
        M = mem0.shape[1]
        scale = Dh ** (-0.5)
        n_seg = S // seg_len
        L = M + seg_len
        idx = torch.arange(L)
        mask = (idx[:, None] < M) | (idx[:, None] >= idx[None, :])
        mem = mem0
        outs = []
        for s in range(n_seg):
            xs = x[:, s * seg_len:(s + 1) * seg_len, :].clone()
            h = torch.concatenate([mem, xs], axis=1)
            q = torch.einsum('btd,dhe->bhte', h, Wq)
            k = torch.einsum('btd,dhe->bhte', h, Wk)
            v = torch.einsum('btd,dhe->bhte', h, Wv)
            scores = torch.einsum('bhqe,bhke->bhqk', q, k) * scale
            scores = torch.where(mask[None, None], scores, -1000000000.0)
            p = _softmax(scores, axis=-1)
            o = torch.einsum('bhqk,bhke->bhqe', p, v)
            o = torch.einsum('bhte,hed->btd', o, Wo)
            mem = o[:, :M, :].clone()
            outs.append(o[:, M:, :])
        return torch.concatenate(outs, axis=1)

    def _B_ref(x, w_gate, w_up, w_down):
        return _gelu(x @ w_gate) * (x @ w_up) @ w_down
    x = A_x
    x = x + _A_ref(_layernorm(x, axis=-1), A_mem0, A_Wq, A_Wk, A_Wv, A_Wo, A_seg_len)
    x = x + _B_ref(_layernorm(x, axis=-1), B_w_gate, B_w_up, B_w_down)
    x = x + _A_ref(_layernorm(x, axis=-1), A_mem0, A_Wq, A_Wk, A_Wv, A_Wo, A_seg_len)
    x = x + _B_ref(_layernorm(x, axis=-1), B_w_gate, B_w_up, B_w_down)
    return x
```

```cpp
#include <hip/hip_runtime.h>
#include <hip/hip_bf16.h>
#include <stdint.h>

typedef unsigned short u16;
__device__ __forceinline__ float bf2f(u16 v) { return __uint_as_float(((unsigned)v) << 16); }
__device__ __forceinline__ u16 f2bf(float f) { __hip_bfloat16 b = __float2bfloat16(f); return *reinterpret_cast<u16*>(&b); }
__device__ __forceinline__ float rbf(float f) { return bf2f(f2bf(f)); }

__global__ void k_ln(const u16* x, u16* y, int rows) {
  int row = blockIdx.x; int t = threadIdx.x;
  float v[8]; float s = 0;
  for (int i = 0; i < 8; ++i) { v[i] = bf2f(x[row * 512 + t + 64 * i]); s += v[i]; }
  for (int o = 32; o; o >>= 1) s += __shfl_xor(s, o);
  float mean = s / 512.f; float q = 0;
  for (int i = 0; i < 8; ++i) { float d = v[i] - mean; q += d * d; }
  for (int o = 32; o; o >>= 1) q += __shfl_xor(q, o);
  float mb = rbf(mean), vb = rbf(q / 512.f);
  float den = rbf(sqrtf(rbf(vb + 1e-5f)));
  for (int i = 0; i < 8; ++i) y[row * 512 + t + 64 * i] = f2bf(rbf(v[i] - mb) / den);
}
__global__ void k_gemm(const u16* A, int lda, const u16* B, int ldb, u16* C, int ldc, const u16* R, int M, int N, int K, int mode) {
  __shared__ float As[16][17], Bs[16][17];
  int tx = threadIdx.x, ty = threadIdx.y;
  int r = blockIdx.y * 16 + ty, c = blockIdx.x * 16 + tx;
  float acc = 0;
  for (int k0 = 0; k0 < K; k0 += 16) {
    As[ty][tx] = r < M ? bf2f(A[(size_t)r * lda + k0 + tx]) : 0.f;
    Bs[ty][tx] = bf2f(B[(size_t)(k0 + ty) * ldb + c]);
    __syncthreads();
    for (int k = 0; k < 16; ++k) acc += As[ty][k] * Bs[k][tx];
    __syncthreads();
  }
  if (r < M) {
    if (mode == 0) C[(size_t)r * ldc + c] = f2bf(acc);
    else C[(size_t)r * ldc + c] = f2bf(bf2f(R[(size_t)r * ldc + c]) + rbf(acc));
  }
}
__global__ void k_attn(const u16* q, const u16* k, const u16* v, u16* o) {
  int h = blockIdx.x, i = blockIdx.y, t = threadIdx.x;
  __shared__ float p[40];
  float s = -1e9f;
  if (t < 40) {
    float d = 0;
    for (int e = 0; e < 128; ++e) d += bf2f(q[i * 4096 + h * 128 + e]) * bf2f(k[t * 4096 + h * 128 + e]);
    d = rbf(rbf(d) * 0.08838834764831845f);
    bool m = (i < 8) || (i >= t);
    s = m ? d : rbf(-1e9f);
  }
  float mx = t < 40 ? s : -INFINITY;
  for (int o2 = 32; o2; o2 >>= 1) mx = fmaxf(mx, __shfl_xor(mx, o2));
  float e = t < 40 ? expf(s - mx) : 0.f;
  float sum = e;
  for (int o2 = 32; o2; o2 >>= 1) sum += __shfl_xor(sum, o2);
  if (t < 40) p[t] = rbf(e / sum);
  __syncthreads();
  for (int e2 = t; e2 < 128; e2 += 64) {
    float a = 0;
    for (int j = 0; j < 40; ++j) a += p[j] * bf2f(v[j * 4096 + h * 128 + e2]);
    o[i * 4096 + h * 128 + e2] = f2bf(a);
  }
}
__global__ void k_add(const u16* a, const u16* b, u16* c, int n) {
  int i = blockIdx.x * 256 + threadIdx.x;
  if (i < n) c[i] = f2bf(bf2f(a[i]) + bf2f(b[i]));
}
__global__ void k_geglu(const u16* g, const u16* u, u16* h, int n) {
  int i = blockIdx.x * 256 + threadIdx.x;
  if (i < n) {
    float x = bf2f(g[i]);
    float inner = 0.7978845608028654f * (x + 0.044715f * x * x * x);
    float ge = rbf(0.5f * x * (1.f + tanhf(inner)));
    h[i] = f2bf(ge * bf2f(u[i]));
  }
}

extern "C" void kernel_launch(void* const* d_in, const int* in_sizes, int n_in, void* d_out, int out_size, void* d_ws, size_t ws_size, hipStream_t stream) {
  const u16* x = (const u16*)d_in[0]; const u16* mem0 = (const u16*)d_in[1];
  const u16* Wq = (const u16*)d_in[2]; const u16* Wk = (const u16*)d_in[3]; const u16* Wv = (const u16*)d_in[4]; const u16* Wo = (const u16*)d_in[5];
  const u16* wg = (const u16*)d_in[8]; const u16* wu = (const u16*)d_in[9]; const u16* wd = (const u16*)d_in[10];
  u16* out = (u16*)d_out;
  u16* ws = (u16*)d_ws;
  u16* xn = ws; ws += 2048 * 512;
  u16* xa = ws; ws += 2048 * 512;
  u16* xb = ws; ws += 2048 * 512;
  u16* aout = ws; ws += 2048 * 512;
  u16* hbuf = ws; ws += 48 * 512;
  u16* obuf = ws; ws += 48 * 512;
  u16* q = ws; ws += 48 * 4096; u16* k = ws; ws += 48 * 4096; u16* v = ws; ws += 48 * 4096; u16* o = ws; ws += 48 * 4096;
  u16* g = ws; ws += 2048 * 1792; u16* u = ws; ws += 2048 * 1792; u16* hh = ws; ws += 2048 * 1792;
  const u16* cur = x;
  for (int layer = 0; layer < 2; ++layer) {
    k_ln<<<2048, 64, 0, stream>>>(cur, xn, 2048);
    hipMemcpyAsync(hbuf, mem0, 8 * 512 * 2, hipMemcpyDeviceToDevice, stream);
    for (int s = 0; s < 64; ++s) {
      hipMemcpyAsync(hbuf + 8 * 512, xn + s * 32 * 512, 32 * 512 * 2, hipMemcpyDeviceToDevice, stream);
      dim3 blk(16, 16);
      k_gemm<<<dim3(256, 3), blk, 0, stream>>>(hbuf, 512, Wq, 4096, q, 4096, nullptr, 40, 4096, 512, 0);
      k_gemm<<<dim3(256, 3), blk, 0, stream>>>(hbuf, 512, Wk, 4096, k, 4096, nullptr, 40, 4096, 512, 0);
      k_gemm<<<dim3(256, 3), blk, 0, stream>>>(hbuf, 512, Wv, 4096, v, 4096, nullptr, 40, 4096, 512, 0);
      k_attn<<<dim3(32, 40), 64, 0, stream>>>(q, k, v, o);
      k_gemm<<<dim3(32, 3), blk, 0, stream>>>(o, 4096, Wo, 512, obuf, 512, nullptr, 40, 512, 4096, 0);
      hipMemcpyAsync(hbuf, obuf, 8 * 512 * 2, hipMemcpyDeviceToDevice, stream);
      hipMemcpyAsync(aout + s * 32 * 512, obuf + 8 * 512, 32 * 512 * 2, hipMemcpyDeviceToDevice, stream);
    }
    k_add<<<4096, 256, 0, stream>>>(cur, aout, xa, 2048 * 512);
    k_ln<<<2048, 64, 0, stream>>>(xa, xn, 2048);
    dim3 blk(16, 16);
    k_gemm<<<dim3(112, 128), blk, 0, stream>>>(xn, 512, wg, 1792, g, 1792, nullptr, 2048, 1792, 512, 0);
    k_gemm<<<dim3(112, 128), blk, 0, stream>>>(xn, 512, wu, 1792, u, 1792, nullptr, 2048, 1792, 512, 0);
    k_geglu<<<2048 * 1792 / 256, 256, 0, stream>>>(g, u, hh, 2048 * 1792);
    u16* dst = layer == 0 ? xb : out;
    k_gemm<<<dim3(32, 128), blk, 0, stream>>>(hh, 1792, wd, 512, dst, 512, xa, 2048, 512, 1792, 1);
    cur = xb;
  }
}
```

```cpp
#include <hip/hip_runtime.h>
#include <hip/hip_bf16.h>
#include <stdint.h>

typedef unsigned short u16;
typedef short bf16x8 __attribute__((ext_vector_type(8)));
typedef float f32x4 __attribute__((ext_vector_type(4)));
typedef float f32x2 __attribute__((ext_vector_type(2)));
typedef unsigned u32x4 __attribute__((ext_vector_type(4)));
typedef unsigned u32x2 __attribute__((ext_vector_type(2)));
typedef __bf16 bfx2 __attribute__((ext_vector_type(2)));

constexpr unsigned O_FXN  = 0;
constexpr unsigned O_FPA  = O_FXN + 16384;
constexpr unsigned O_FXN1 = O_FPA + 16384;
constexpr unsigned O_FPF  = O_FXN1 + 16384;
constexpr unsigned O_ABT  = O_FPF + 16384;
constexpr unsigned O_FH   = O_ABT + 256;
constexpr unsigned O_MEM  = O_FH + 262144;
constexpr unsigned O_PC   = O_MEM + 1048576;
constexpr unsigned CTRL_BYTES = O_PC + 1048576;
constexpr unsigned O_WQKV = (CTRL_BYTES + 4095) & ~4095u;
constexpr unsigned O_WO   = O_WQKV + 3 * 512 * 4096 * 2;
constexpr unsigned O_WGU  = O_WO + 4096 * 512 * 2;
constexpr unsigned O_WD   = O_WGU + 2 * 512 * 1792 * 2;
constexpr unsigned O_XN   = O_WD + 1792 * 512 * 2;
constexpr unsigned O_XN1  = O_XN + 2 * 64 * 32768;
constexpr unsigned O_Q    = O_XN1 + 2 * 64 * 32768;
constexpr unsigned QKV_SZ = 64 * 32 * 8192;
constexpr unsigned O_KM   = O_Q + 3 * QKV_SZ;
constexpr unsigned O_VMT  = O_KM + 2 * 64 * 32 * 2048;
constexpr unsigned O_PA   = O_VMT + 2 * 64 * 32 * 2048;
constexpr unsigned O_PF   = O_PA + 64u * 16 * 65536;
constexpr unsigned WS_END = O_PF + 64u * 14 * 65536;
static_assert(WS_END <= 256u * 1024 * 1024, "workspace");

constexpr int LDS_BYTES = 160 * 1024;
#define SC1 16
#define OOB 0x7fffff00u
#define EMPTY 0xffffffffu
#define SPIN_CAP (1u << 20)
#define DELAY_A 20
#define DELAY_B 12

#define SB() __builtin_amdgcn_sched_barrier(0)
#define MFMA(a, b, c) __builtin_amdgcn_mfma_f32_16x16x32_bf16(a, b, c, 0, 0, 0)
#define BC8(x) __builtin_bit_cast(bf16x8, x)
#define MA0(acc, A, B) asm volatile("v_mfma_f32_16x16x32_bf16 %0, %1, %2, 0" : "=v"(acc) : "a"(A), "v"(B))
#define MA1(acc, A, B) asm volatile("v_mfma_f32_16x16x32_bf16 %0, %1, %2, %0" : "+v"(acc) : "a"(A), "v"(B))
#define MV0(acc, A, B) asm volatile("v_mfma_f32_16x16x32_bf16 %0, %1, %2, 0" : "=v"(acc) : "v"(A), "v"(B))
#define MV1(acc, A, B) asm volatile("v_mfma_f32_16x16x32_bf16 %0, %1, %2, %0" : "+v"(acc) : "v"(A), "v"(B))
#define NOP(n) asm volatile("s_nop %0" :: "i"(n))
#define PINA(x) asm volatile("" : "+a"(x))
#define DPP8(x) ((unsigned)__builtin_amdgcn_update_dpp(0, (int)(x), 0x128, 0xf, 0xf, false))
#define DRAIN() asm volatile("s_waitcnt vmcnt(0)" ::: "memory")
#define MEMFENCE() asm volatile("" ::: "memory")
#define LBAR() do { asm volatile("s_waitcnt lgkmcnt(0)" ::: "memory"); __builtin_amdgcn_s_barrier(); asm volatile("" ::: "memory"); } while (0)
#define LD128(off, so) __builtin_amdgcn_raw_buffer_load_b128(ws, (off), (so), SC1)
#define LD64(off, so) __builtin_amdgcn_raw_buffer_load_b64(ws, (off), (so), SC1)
#define LD32(off, so) __builtin_amdgcn_raw_buffer_load_b32(ws, (off), (so), SC1)
#define ST128(v, off, so) __builtin_amdgcn_raw_buffer_store_b128((v), ws, (off), (so), SC1)
#define ST64(v, off, so) __builtin_amdgcn_raw_buffer_store_b64((v), ws, (off), (so), SC1)
#define ST32(v, off, so) __builtin_amdgcn_raw_buffer_store_b32((v), ws, (off), (so), SC1)
typedef __amdgpu_buffer_rsrc_t rsrc_t;

__device__ __forceinline__ unsigned pk(float a, float b) { f32x2 v = {a, b}; bfx2 r = __builtin_convertvector(v, bfx2); return __builtin_bit_cast(unsigned, r); }
__device__ __forceinline__ float rb(float a) { return __uint_as_float(pk(a, a) << 16); }
__device__ __forceinline__ float lo16(unsigned u) { return __uint_as_float(u << 16); }
__device__ __forceinline__ float hi16(unsigned u) { return __uint_as_float(u & 0xffff0000u); }
__device__ __forceinline__ float red_max(float x) {
  auto r = __builtin_amdgcn_permlane32_swap(__float_as_uint(x), __float_as_uint(x), false, false);
  x = fmaxf(__uint_as_float(r[0]), __uint_as_float(r[1]));
  auto q = __builtin_amdgcn_permlane16_swap(__float_as_uint(x), __float_as_uint(x), false, false);
  return fmaxf(__uint_as_float(q[0]), __uint_as_float(q[1]));
}
__device__ __forceinline__ float red_sum(float x) {
  auto r = __builtin_amdgcn_permlane32_swap(__float_as_uint(x), __float_as_uint(x), false, false);
  x = __uint_as_float(r[0]) + __uint_as_float(r[1]);
  auto q = __builtin_amdgcn_permlane16_swap(__float_as_uint(x), __float_as_uint(x), false, false);
  return __uint_as_float(q[0]) + __uint_as_float(q[1]);
}
__device__ __forceinline__ float wave_sum(float x) {
  x += __uint_as_float((unsigned)__builtin_amdgcn_update_dpp(0, (int)__float_as_uint(x), 0x121, 0xf, 0xf, false));
  x += __uint_as_float((unsigned)__builtin_amdgcn_update_dpp(0, (int)__float_as_uint(x), 0x122, 0xf, 0xf, false));
  x += __uint_as_float((unsigned)__builtin_amdgcn_update_dpp(0, (int)__float_as_uint(x), 0x124, 0xf, 0xf, false));
  x += __uint_as_float(DPP8(__float_as_uint(x)));
  return red_sum(x);
}

__device__ __forceinline__ bool wait_flags(rsrc_t ws, unsigned off, int n, int lane) {
  const unsigned vo = lane < n ? (unsigned)lane * 4u : OOB;
  for (unsigned spins = 0; spins < SPIN_CAP; ++spins) {
    MEMFENCE();
    unsigned v = LD32(vo, off);
    if (__all(v != EMPTY)) return true;
    __builtin_amdgcn_s_sleep(2);
    if ((spins & 63u) == 63u) { if (__builtin_amdgcn_readfirstlane(LD32(0u, O_ABT)) == 1u) return false; }
  }
  if (lane == 0) ST32(1u, 0u, O_ABT);
  return false;
}

struct Params {
  const u16* x; const u16* mem0; u16* out; char* wsp; int role_mask; int layer_mask;
};

__global__ void k_prep(const u16* Wq, const u16* Wk, const u16* Wv, const u16* Wo, const u16* wg, const u16* wu, const u16* wd, char* wsp) {
  int idx = blockIdx.x * 256 + threadIdx.x;
  const int N1 = 32 * 4 * 3 * 2 * 16 * 64, N2 = 32 * 32 * 4 * 64, N3 = 14 * 4 * 2 * 2 * 16 * 64, N4 = 14 * 32 * 4 * 64;
  const int lane = idx & 63, r = lane & 15, g = lane >> 4;
  const u16* src; size_t ld; u16* dst;
  if (idx < N1) {
    int kk = (idx >> 6) & 15, t = (idx >> 10) & 1; int r2 = idx >> 11; int m = r2 % 3; r2 /= 3; int w = r2 & 3, h = r2 >> 2;
    const u16* W = m == 0 ? Wq : (m == 1 ? Wk : Wv);
    src = W + (size_t)(32 * kk + 8 * g) * 4096 + h * 128 + 32 * w + 8 * (r >> 2) + 4 * t + (r & 3); ld = 4096;
    dst = (u16*)(wsp + O_WQKV) + (size_t)idx * 8;
  } else if (idx < N1 + N2) {
    int id = idx - N1; int ks = (id >> 6) & 3, ct = (id >> 8) & 31, h = id >> 13;
    src = Wo + (size_t)(h * 128 + 32 * ks + 8 * g) * 512 + 16 * ct + r; ld = 512;
    dst = (u16*)(wsp + O_WO) + (size_t)id * 8;
  } else if (idx < N1 + N2 + N3) {
    int id = idx - N1 - N2; int kk = (id >> 6) & 15, t = (id >> 10) & 1, m = (id >> 11) & 1, w = (id >> 12) & 3, f = id >> 14;
    const u16* W = m == 0 ? wg : wu;
    src = W + (size_t)(32 * kk + 8 * g) * 1792 + f * 128 + 32 * w + 8 * (r >> 2) + 4 * t + (r & 3); ld = 1792;
    dst = (u16*)(wsp + O_WGU) + (size_t)id * 8;
  } else if (idx < N1 + N2 + N3 + N4) {
    int id = idx - N1 - N2 - N3; int ks = (id >> 6) & 3, ct = (id >> 8) & 31, f = id >> 13;
    src = wd + (size_t)(f * 128 + 32 * ks + 8 * g) * 512 + 16 * ct + r; ld = 512;
    dst = (u16*)(wsp + O_WD) + (size_t)id * 8;
  } else return;
  u32x4 o;
  o.x = (unsigned)src[0] | ((unsigned)src[ld] << 16); o.y = (unsigned)src[2 * ld] | ((unsigned)src[3 * ld] << 16);
  o.z = (unsigned)src[4 * ld] | ((unsigned)src[5 * ld] << 16); o.w = (unsigned)src[6 * ld] | ((unsigned)src[7 * ld] << 16);
  *(u32x4*)dst = o;
}

__device__ __forceinline__ void role_chain(const Params& p, rsrc_t ws, char* lds, const int l, const int h) {
  const int tid = threadIdx.x, lane = tid & 63, w = __builtin_amdgcn_readfirstlane(tid >> 6), i = lane & 15, g = lane >> 4;
  const int vi = g * 8 + (i & 7);
  bf16x8* ldsWo = (bf16x8*)lds;
  bf16x8* memf = (bf16x8*)(lds + 131072);
  f32x4* sc = (f32x4*)(lds + 131072 + 8192);
  bf16x8* of = (bf16x8*)(lds + 131072 + 8192 + 7168);
  bf16x8* wx = (bf16x8*)(lds + 131072 + 8192 + 7168 + 2048) + w * 192 + lane;
  bf16x8 wf[3][2][16];
  {
    const bf16x8* src = (const bf16x8*)(p.wsp + O_WQKV) + (size_t)((h * 4 + w) * 96) * 64 + lane;
    #pragma unroll
    for (int m = 0; m < 3; ++m)
    #pragma unroll
    for (int t = 0; t < 2; ++t) {
      #pragma unroll
      for (int kk = 0; kk < 16; ++kk) { wf[m][t][kk] = src[((m * 2 + t) * 16 + kk) * 64]; }
      if (m == 2) { wx[(t * 2 + 0) * 64] = wf[m][t][14]; if (t == 0) wx[64] = wf[m][t][15]; }
      SB();
      #pragma unroll
      for (int kk = 0; kk < 16; ++kk) { if (m < 2) PINA(wf[m][t][kk]); }
      SB();
    }
    const bf16x8* so = (const bf16x8*)(p.wsp + O_WO) + (size_t)h * 8192;
    for (int k = 0; k < 32; ++k) ldsWo[k * 256 + tid] = so[k * 256 + tid];
  }
  rsrc_t rM0 = __builtin_amdgcn_make_buffer_rsrc((void*)p.mem0, 0, 8192, 0x00020000);
  const int prm = 8 * (i >> 2) + (i & 3);
  const int kOff = (prm * 128 + 32 * w + 8 * g) * 2;
  const int vOff = ((32 * w + prm) * 32 + 8 * g) * 2;
  const unsigned oK = O_Q + QKV_SZ + h * 8192, oVT = O_Q + 2 * QKV_SZ + h * 8192;
  const unsigned oFH = O_FH + (l * 64 * 32 + h) * 64;
  const unsigned oKM = O_KM + (l * 64 * 32 + h) * 2048, oVMT = O_VMT + (l * 64 * 32 + h) * 2048;
  const unsigned oMEM = O_MEM + l * 64 * 8192, oPC = O_PC + l * 32 * 32 * 512;
  u32x4 kx[2], vx[2];
  if (!wait_flags(ws, oFH + 4, 2, lane)) return;
  #pragma unroll
  for (int t = 0; t < 2; ++t) { kx[t] = LD128(kOff + t * 1024, oK); vx[t] = LD128(vOff + t * 256, oVT); }
  __syncthreads();
  const int hi = i >> 3;
  const bf16x8* mfp = memf + g * 8 + (i & 7);
  const bf16x8* wop = ldsWo + w * 2048 + lane;
  const f32x4* scx = sc + hi * 32 + vi;
  const f32x2* scm = (const f32x2*)(sc + 64 + vi) + hi;
  f32x4* scw = sc + w * 96 + vi;
  u32x4* ofw = hi ? (u32x4*)(sc) + 384 + lane : (u32x4*)of + w * 32 + vi;
  const bf16x8* ofr_p = of + vi;
  const int tid16 = tid * 16;
  const unsigned pcwOff = vi * 16 + hi * 16384;
  const unsigned kmOff = hi ? OOB : (unsigned)((i * 128 + 32 * w + 8 * g) * 2);
  const unsigned vmOff = g < 2 ? (unsigned)(((32 * w + prm) * 8 + 4 * g) * 2) : OOB;
  const int pcrOff = (lane >> 3) * 2048 + (w * 8 + (lane & 7)) * 16;
  const unsigned memOff = lane < 8 ? (unsigned)(lane * 32 + w * 8) : OOB;
  const unsigned flOff = lane < 2 ? (unsigned)(lane * 4 + 4) : OOB;
  const int mfw = (((tid >> 5) * 4 + ((tid >> 4) & 1) * 2 + (tid & 1)) * 8 + ((tid >> 1) & 7));
  const float cexp = 0.08838834764831845f * 1.4426950408889634f;
  for (int s = 0; s < 64; ++s) {
    {
      u32x4 x[2];
      if (s == 0) {
        #pragma unroll
        for (int k = 0; k < 2; ++k) { int c = k * 256 + tid; x[k] = __builtin_amdgcn_raw_buffer_load_b128(rM0, (((c >> 1) & 7) * 512 + (c >> 4) * 16 + (c & 1) * 8) * 2, 0, 0); }
      } else {
        __builtin_amdgcn_s_sleep(DELAY_B);
        unsigned spins = 0;
        for (; spins < SPIN_CAP; ++spins) {
          MEMFENCE();
          #pragma unroll
          for (int k = 0; k < 2; ++k) x[k] = LD128(tid16, oMEM + s * 8192 + k * 4096);
          bool ok = true;
          #pragma unroll
          for (int k = 0; k < 2; ++k) ok &= (x[k].x != EMPTY) && (x[k].y != EMPTY) && (x[k].z != EMPTY) && (x[k].w != EMPTY);
          if (__all(ok)) break;
        }
        if (spins >= SPIN_CAP && lane == 0) ST32(1u, 0u, O_ABT);
      }
      #pragma unroll
      for (int k = 0; k < 2; ++k) ((u32x4*)memf)[mfw + k * 256] = x[k];
    }
    LBAR();
    if (s > 0 && tid == 0) ST32(1u, 12u, oFH + (s - 1) * 2048);
    f32x4 aq[2], ak[2], av[2];
    bf16x8 mf[8];
    #pragma unroll
    for (int kk = 0; kk < 8; ++kk) mf[kk] = mfp[kk * 32];
    SB();
    #pragma unroll
    for (int kk = 0; kk < 16; ++kk) {
      #pragma unroll
      for (int t = 0; t < 2; ++t) {
        if (kk == 0) { MA0(aq[t], wf[0][t][kk], mf[kk & 7]); MA0(ak[t], wf[1][t][kk], mf[kk & 7]); }
        else { MA1(aq[t], wf[0][t][kk], mf[kk & 7]); MA1(ak[t], wf[1][t][kk], mf[kk & 7]); }
      }
      SB();
      mf[kk & 7] = mfp[((kk + 8) & 15) * 32];
      SB();
    }
#define V1(kk, t) do { if ((kk) == 0) MV0(av[t], mf[(kk) & 7], wf[2][t][kk]); else MV1(av[t], mf[(kk) & 7], wf[2][t][kk]); SB(); } while (0)
#define VRL(kk) do { mf[(kk) & 7] = mfp[((kk) + 8) * 32]; SB(); } while (0)
    V1(0, 0); V1(0, 1); VRL(0); NOP(1); SB();
    u32x4 qf, kf;
    qf.x = pk(aq[0][0], aq[0][1]); qf.y = pk(aq[0][2], aq[0][3]); SB();
    V1(1, 0);
    qf.z = pk(aq[1][0], aq[1][1]); qf.w = pk(aq[1][2], aq[1][3]); SB();
    V1(1, 1); VRL(1);
    kf.x = pk(ak[0][0], ak[0][1]); kf.y = pk(ak[0][2], ak[0][3]); SB();
    f32x4 s0, s1, s2;
    MV0(s0, kx[0], qf); SB();
    kf.z = pk(ak[1][0], ak[1][1]); kf.w = pk(ak[1][2], ak[1][3]); SB();
    MV0(s1, kx[1], qf); SB();
    NOP(0); SB();
    MV0(s2, kf, qf); SB();
    V1(2, 0); V1(2, 1); VRL(2); V1(3, 0);
    scw[0] = s0; SB();
    V1(3, 1); VRL(3);
    scw[32] = s1; SB(); scw[64] = s2; SB();
    LBAR();
    f32x4 xa = scx[0], xb = scx[96], xc = scx[192], xd = scx[288];
    f32x2 ma = scm[0], mb = scm[192], mc = scm[384], md = scm[576];
    SB();
    V1(4, 0);
    ST128(kf, kmOff, oKM + s * 65536); SB();
    V1(4, 1); VRL(4); V1(5, 0);
    unsigned nfl = LD32(flOff, oFH + ((s + 1) & 63) * 2048); SB();
    V1(5, 1); VRL(5); V1(6, 0); V1(6, 1); VRL(6);
    xa = xa + xb; SB();
    V1(7, 0);
    xc = xc + xd; SB();
    V1(7, 1); VRL(7);
    xa = xa + xc; ma = ma + mb; SB();
    V1(8, 0);
    mc = mc + md; ma = ma + mc; SB();
    V1(8, 1);
    if (g >= 2) { ma[0] = -INFINITY; ma[1] = -INFINITY; }
    float mx = fmaxf(fmaxf(xa[0], xa[1]), fmaxf(xa[2], xa[3])); SB();
    V1(9, 0);
    mx = fmaxf(mx, fmaxf(ma[0], ma[1])); SB();
    V1(9, 1);
    mx = fmaxf(mx, __uint_as_float(DPP8(__float_as_uint(mx)))); SB();
    V1(10, 0);
    { auto r = __builtin_amdgcn_permlane32_swap(__float_as_uint(mx), __float_as_uint(mx), false, false); mx = fmaxf(__uint_as_float(r[0]), __uint_as_float(r[1])); } SB();
    V1(10, 1);
    { auto r = __builtin_amdgcn_permlane16_swap(__float_as_uint(mx), __float_as_uint(mx), false, false); mx = fmaxf(__uint_as_float(r[0]), __uint_as_float(r[1])); }
    float nm = -mx * cexp; SB();
    V1(11, 0);
    float e0 = __builtin_amdgcn_exp2f(fmaf(xa[0], cexp, nm)), e1 = __builtin_amdgcn_exp2f(fmaf(xa[1], cexp, nm)); SB();
    V1(11, 1);
    float e2 = __builtin_amdgcn_exp2f(fmaf(xa[2], cexp, nm)), e3 = __builtin_amdgcn_exp2f(fmaf(xa[3], cexp, nm)); SB();
    V1(12, 0);
    wf[2][0][14] = wx[0]; wf[2][1][14] = wx[128]; SB();
    float e4 = __builtin_amdgcn_exp2f(fmaf(ma[0], cexp, nm)), e5 = __builtin_amdgcn_exp2f(fmaf(ma[1], cexp, nm)); SB();
    V1(12, 1);
    wf[2][0][15] = wx[64]; SB();
    float sum = (e0 + e1) + (e2 + e3); SB();
    V1(13, 0);
    sum += e4 + e5; SB();
    V1(13, 1);
    sum += __uint_as_float(DPP8(__float_as_uint(sum))); SB();
    V1(14, 0);
    { auto r = __builtin_amdgcn_permlane32_swap(__float_as_uint(sum), __float_as_uint(sum), false, false); sum = __uint_as_float(r[0]) + __uint_as_float(r[1]); } SB();
    V1(14, 1);
    { auto r = __builtin_amdgcn_permlane16_swap(__float_as_uint(sum), __float_as_uint(sum), false, false); sum = __uint_as_float(r[0]) + __uint_as_float(r[1]); } SB();
    V1(15, 0);
    float inv = __builtin_amdgcn_rcpf(sum); SB();
    V1(15, 1);
    bf16x8 wo[16];
    unsigned p0 = pk(e0 * inv, e1 * inv), p1 = pk(e2 * inv, e3 * inv), p2 = pk(e4 * inv, e5 * inv);
    u32x4 pf = {p0, p1, DPP8(p0), DPP8(p1)};
    u32x4 pm4 = {p2, DPP8(p2), 0u, 0u};
    SB();
    f32x4 ao[2];
    NOP(1); SB();
    MV0(ao[0], vx[0], pf); SB();
    u32x4 vm0 = {pk(av[0][0], av[0][1]), pk(av[0][2], av[0][3]), 0u, 0u}; SB();
    MV0(ao[1], vx[1], pf); SB();
    u32x4 vm1 = {pk(av[1][0], av[1][1]), pk(av[1][2], av[1][3]), 0u, 0u}; SB();
    MV1(ao[0], vm0, pm4); SB();
    NOP(1); SB();
    MV1(ao[1], vm1, pm4); SB();
    #pragma unroll
    for (int k = 0; k < 8; ++k) wo[k] = wop[k * 64];
    SB();
    u32x4 ofr = {pk(ao[0][0], ao[0][1]), pk(ao[0][2], ao[0][3]), pk(ao[1][0], ao[1][1]), pk(ao[1][2], ao[1][3])};
    *ofw = ofr;
    SB();
    #pragma unroll
    for (int k = 8; k < 16; ++k) wo[k] = wop[k * 64];
    { u32x2 t0 = {vm0.x, vm0.y}, t1 = {vm1.x, vm1.y};
      ST64(t0, vmOff, oVMT + s * 65536);
      ST64(t1, vmOff + 64, oVMT + s * 65536); }
    if (s == 63) break;
    SB();
    asm volatile("s_waitcnt lgkmcnt(8)" ::: "memory");
    __builtin_amdgcn_s_barrier();
    MEMFENCE();
    bf16x8 ob[4];
    #pragma unroll
    for (int ks = 0; ks < 4; ++ks) ob[ks] = ofr_p[ks * 32];
    SB();
    const unsigned tag = s & 1;
    f32x4 accA[2], accB[2];
    #pragma unroll
    for (int pr = 0; pr < 5; ++pr) {
      u32x4 o;
      #pragma unroll
      for (int ks = 0; ks < 4; ++ks) {
        if (pr < 4) { if (ks == 0) MV0(accA[pr & 1], wo[(pr & 1) * 8 + ks], ob[ks]); else MV1(accA[pr & 1], wo[(pr & 1) * 8 + ks], ob[ks]); SB(); }
        if (pr < 4) { if (ks == 0) MV0(accB[pr & 1], wo[(pr & 1) * 8 + 4 + ks], ob[ks]); else MV1(accB[pr & 1], wo[(pr & 1) * 8 + 4 + ks], ob[ks]); SB(); }
        if (pr == 4 && ks == 0) { NOP(7); SB(); }
        if (pr > 0 && (ks > 1 || pr == 4)) {
          #pragma unroll
          for (int e = (pr == 4 ? ks : (ks - 2) * 2); e < (pr == 4 ? ks + 1 : (ks - 2) * 2 + 2); ++e) { float v = hi ? accB[(pr - 1) & 1][e] : accA[(pr - 1) & 1][e]; o[e] = (__float_as_uint(v) & ~1u) | tag; }
          SB(); }
        if (pr < 2) { wo[(pr & 1) * 8 + ks] = wop[((pr + 2) * 8 + ks) * 64]; wo[(pr & 1) * 8 + 4 + ks] = wop[((pr + 2) * 8 + 4 + ks) * 64]; SB(); }
      }
      if (pr > 0) { ST128(o, pcwOff, oPC + ((w * 8 + 2 * (pr - 1)) * 32 + h) * 512); SB(); }
    }
    if (!__all(nfl != EMPTY)) { if (!wait_flags(ws, oFH + (s + 1) * 2048 + 4, 2, lane)) {   } }
    #pragma unroll
    for (int t = 0; t < 2; ++t) { kx[t] = LD128(kOff + t * 1024, oK + (s + 1) * 262144); vx[t] = LD128(vOff + t * 256, oVT + (s + 1) * 262144); }
    SB();
    {
      __builtin_amdgcn_s_sleep(DELAY_A);
      u32x4 x[4];
      unsigned spins = 0;
      for (; spins < SPIN_CAP; ++spins) {
        MEMFENCE();
        #pragma unroll
        for (int k = 0; k < 4; ++k) x[k] = LD128(pcrOff + k * 512, oPC + h * 16384);
        unsigned an = 1, orr = 0;
        #pragma unroll
        for (int k = 0; k < 4; ++k) { an &= x[k].x & x[k].y & x[k].z & x[k].w; orr |= x[k].x | x[k].y | x[k].z | x[k].w; }
        bool ok = tag ? (an & 1) : !(orr & 1);
        if (__all(ok)) break;
      }
      if (spins >= SPIN_CAP && lane == 0) ST32(1u, 0u, O_ABT);
      DRAIN();
      f32x4 a = (__builtin_bit_cast(f32x4, x[0]) + __builtin_bit_cast(f32x4, x[1])) + (__builtin_bit_cast(f32x4, x[2]) + __builtin_bit_cast(f32x4, x[3]));
      #pragma unroll
      for (int e = 0; e < 4; ++e) {
        float v = a[e];
        v += __uint_as_float(DPP8(__float_as_uint(v)));
        a[e] = red_sum(v);
      }
      u32x2 o = {pk(a[0], a[1]), pk(a[2], a[3])};
      if (o.x == EMPTY) o.x = 0xfffefffeu;
      if (o.y == EMPTY) o.y = 0xfffefffeu;
      ST64(o, memOff, oMEM + (s + 1) * 8192 + 256 * h);
    }
  }
  DRAIN();
  __syncthreads();
  if (tid == 0) ST32(1u, 12u, oFH + 63 * 2048);
#undef V1
#undef VRL
}

__device__ __forceinline__ void stage_x(u32x4* lx, const u32x4 (&xr)[8], int tid) {
  const int c16 = tid & 63, tb = tid >> 6;
  #pragma unroll
  for (int k = 0; k < 8; ++k) lx[c16 * 32 + ((4 * k + tb) ^ (c16 & 31))] = xr[k];
}

__device__ __forceinline__ void role_qkv(const Params& p, rsrc_t ws, char* lds, const int m, const int h) {
  const int tid = threadIdx.x, lane = tid & 63, w = __builtin_amdgcn_readfirstlane(tid >> 6), i = lane & 15, g = lane >> 4;
  u32x4* lx = (u32x4*)lds;
  volatile int* dec = (volatile int*)(lds + 32768);
  bf16x8 wf[2][16];
  {
    const bf16x8* src = (const bf16x8*)(p.wsp + O_WQKV) + (size_t)(((h * 4 + w) * 3 + m) * 32) * 64 + lane;
    #pragma unroll
    for (int t = 0; t < 2; ++t)
    #pragma unroll
    for (int kk = 0; kk < 16; ++kk) wf[t][kk] = src[(t * 16 + kk) * 64];
  }
  const int prm = 8 * (i >> 2) + (i & 3);
  const int tok0 = m == 2 ? prm : i, tokS = m == 2 ? 4 : 16;
  int n0 = (p.layer_mask & 1) ? 0 : 64, n1 = (p.layer_mask & 2) ? 0 : 64;
  const unsigned flv = lane < 32 ? (unsigned)lane * 4u : OOB;
  u32x4 xr[8];
  int cur = -1;
  auto pick_blocking = [&]() -> int {
    if (w == 0) {
      int d = -2;
      for (unsigned spins = 0; spins < SPIN_CAP; ++spins) {
        MEMFENCE();
        unsigned f1 = n1 < 64 ? LD32(flv, O_FXN + (64 + n1) * 128) : EMPTY;
        unsigned f0 = n0 < 64 ? LD32(flv, O_FXN + n0 * 128) : EMPTY;
        if (__all(f1 != EMPTY)) { d = 64 + n1; break; }
        if (__all(f0 != EMPTY)) { d = n0; break; }
        __builtin_amdgcn_s_sleep(2);
        if ((spins & 63u) == 63u) { if (__builtin_amdgcn_readfirstlane(LD32(0u, O_ABT)) == 1u) break; }
      }
      if (d == -2 && lane == 0) ST32(1u, 0u, O_ABT);
      if (lane == 0) *dec = d;
    }
    __syncthreads();
    int d = *dec;
    __syncthreads();
    return __builtin_amdgcn_readfirstlane(d);
  };
  auto issue = [&](int item) {
    #pragma unroll
    for (int k = 0; k < 8; ++k) xr[k] = LD128(tid * 16, O_XN + item * 32768 + k * 4096);
    if (item >= 64) ++n1; else ++n0;
  };
  if (n0 >= 64 && n1 >= 64) return;
  cur = pick_blocking();
  if (cur < 0) return;
  issue(cur);
  for (;;) {
    unsigned f1 = EMPTY, f0 = EMPTY;
    if (w == 0) {
      if (n1 < 64) f1 = LD32(flv, O_FXN + (64 + n1) * 128);
      if (n0 < 64) f0 = LD32(flv, O_FXN + n0 * 128);
    }
    stage_x(lx, xr, tid);
    if (w == 0) { int d = __all(f1 != EMPTY) ? 64 + n1 : (__all(f0 != EMPTY) ? n0 : -1); if (lane == 0) *dec = d; }
    __syncthreads();
    int nxt = __builtin_amdgcn_readfirstlane(*dec);
    if (nxt >= 0) issue(nxt);
    f32x4 acc[2][2];
    #pragma unroll
    for (int kk = 0; kk < 16; ++kk) {
      const int c16 = kk * 4 + g;
      bf16x8 x0 = BC8(lx[c16 * 32 + (tok0 ^ (c16 & 31))]), x1 = BC8(lx[c16 * 32 + ((tok0 + tokS) ^ (c16 & 31))]);
      const f32x4 z = {0, 0, 0, 0};
      #pragma unroll
      for (int t = 0; t < 2; ++t) {
        if (m == 2) { acc[t][0] = MFMA(x0, wf[t][kk], kk ? acc[t][0] : z); acc[t][1] = MFMA(x1, wf[t][kk], kk ? acc[t][1] : z); }
        else { acc[t][0] = MFMA(wf[t][kk], x0, kk ? acc[t][0] : z); acc[t][1] = MFMA(wf[t][kk], x1, kk ? acc[t][1] : z); }
      }
    }
    const int s = cur & 63, l = cur >> 6;
    const unsigned ob = O_Q + m * QKV_SZ + (s * 32 + h) * 8192;
    if (m == 2) {
      #pragma unroll
      for (int t = 0; t < 2; ++t) {
        u32x4 o = {pk(acc[t][0][0], acc[t][0][1]), pk(acc[t][0][2], acc[t][0][3]), pk(acc[t][1][0], acc[t][1][1]), pk(acc[t][1][2], acc[t][1][3])};
        ST128(o, ((32 * w + prm + 4 * t) * 32 + 8 * g) * 2, ob);
      }
    } else {
      #pragma unroll
      for (int rt = 0; rt < 2; ++rt) {
        u32x4 o = {pk(acc[0][rt][0], acc[0][rt][1]), pk(acc[0][rt][2], acc[0][rt][3]), pk(acc[1][rt][0], acc[1][rt][1]), pk(acc[1][rt][2], acc[1][rt][3])};
        ST128(o, ((16 * rt + i) * 128 + 32 * w + 8 * g) * 2, ob);
      }
    }
    DRAIN();
    __syncthreads();
    if (tid == 0) ST32(1u, m * 4, O_FH + ((l * 64 + s) * 32 + h) * 64);
    if (nxt < 0) {
      if (n0 >= 64 && n1 >= 64) break;
      nxt = pick_blocking();
      if (nxt < 0) break;
      issue(nxt);
    }
    cur = nxt;
  }
}

__device__ __forceinline__ void role_att(const Params& p, rsrc_t ws, char* lds, const int l, const int a) {
  const int tid = threadIdx.x, lane = tid & 63, w = __builtin_amdgcn_readfirstlane(tid >> 6), i = lane & 15, g = lane >> 4;
  u32x4* ofx = (u32x4*)lds;
  volatile int* okw = (volatile int*)(lds + 16384);
  bf16x8 wo[2][8][4];
  #pragma unroll
  for (int hh = 0; hh < 2; ++hh) {
    const bf16x8* src = (const bf16x8*)(p.wsp + O_WO) + (size_t)((2 * a + hh) * 32 + w * 8) * 256 + lane;
    #pragma unroll
    for (int c = 0; c < 8; ++c)
    #pragma unroll
    for (int ks = 0; ks < 4; ++ks) wo[hh][c][ks] = src[(c * 4 + ks) * 64];
    SB();
    #pragma unroll
    for (int c = 0; c < 8; ++c)
    #pragma unroll
    for (int ks = 0; ks < 4; ++ks) { if (hh * 32 + c * 4 + ks < 44) PINA(wo[hh][c][ks]); }
    SB();
  }
  const int hh = w >> 1, qh = w & 1, h = 2 * a + hh;
  const int prm = 8 * (i >> 2) + (i & 3);
  const int tq = 16 * qh + i;
  const float cexp = 0.08838834764831845f * 1.4426950408889634f;
  const f32x4 z = {0, 0, 0, 0};
  for (int s = 0; s < 64; ++s) {
    if (w == 0) {
      const unsigned vo = lane < 8 ? (unsigned)((lane >> 2) * 64 + (lane & 3) * 4) : OOB;
      bool ok = false;
      for (unsigned spins = 0; spins < SPIN_CAP; ++spins) {
        MEMFENCE();
        unsigned v = LD32(vo, O_FH + ((l * 64 + s) * 32 + 2 * a) * 64);
        if (__all(v != EMPTY)) { ok = true; break; }
        __builtin_amdgcn_s_sleep(2);
        if ((spins & 63u) == 63u) { if (__builtin_amdgcn_readfirstlane(LD32(0u, O_ABT)) == 1u) break; }
      }
      if (!ok && lane == 0) ST32(1u, 0u, O_ABT);
      if (lane == 0) *okw = ok;
    }
    __syncthreads();
    if (!*okw) return;
    const unsigned oq = O_Q + (s * 32 + h) * 8192, okm = ((l * 64 + s) * 32 + h) * 2048;
    u32x4 qf[4], kf[2][4], kmf[4], vf[8]; u32x2 vmf[8];
    #pragma unroll
    for (int ks = 0; ks < 4; ++ks) {
      qf[ks] = LD128((tq * 128 + 32 * ks + 8 * g) * 2, oq);
      kf[0][ks] = LD128((prm * 128 + 32 * ks + 8 * g) * 2, oq + QKV_SZ);
      kf[1][ks] = LD128(((prm + 4) * 128 + 32 * ks + 8 * g) * 2, oq + QKV_SZ);
      kmf[ks] = LD128(((i & 7) * 128 + 32 * ks + 8 * g) * 2, O_KM + okm);
    }
    #pragma unroll
    for (int t8 = 0; t8 < 8; ++t8) {
      const int e = 32 * (t8 >> 1) + prm + 4 * (t8 & 1);
      vf[t8] = LD128((e * 32 + 8 * g) * 2, oq + 2 * QKV_SZ);
      vmf[t8] = LD64((e * 8 + 4 * (g & 1)) * 2, O_VMT + okm);
    }
    f32x4 s0 = z, s1 = z, s2 = z;
    #pragma unroll
    for (int ks = 0; ks < 4; ++ks) {
      s0 = MFMA(BC8(kf[0][ks]), BC8(qf[ks]), s0);
      s1 = MFMA(BC8(kf[1][ks]), BC8(qf[ks]), s1);
      s2 = MFMA(BC8(kmf[ks]), BC8(qf[ks]), s2);
    }
    float sv[12];
    #pragma unroll
    for (int b = 0; b < 4; ++b) {
      sv[b] = (8 * g + b <= tq) ? s0[b] : -INFINITY;
      sv[4 + b] = (8 * g + 4 + b <= tq) ? s1[b] : -INFINITY;
      sv[8 + b] = g < 2 ? s2[b] : -INFINITY;
    }
    float mx = sv[0];
    #pragma unroll
    for (int b = 1; b < 12; ++b) mx = fmaxf(mx, sv[b]);
    mx = red_max(mx);
    const float nm = -mx * cexp;
    float sum = 0;
    #pragma unroll
    for (int b = 0; b < 12; ++b) { sv[b] = __builtin_amdgcn_exp2f(fmaf(sv[b], cexp, nm)); sum += sv[b]; }
    sum = red_sum(sum);
    const float inv = __builtin_amdgcn_rcpf(sum);
    u32x4 pf = {pk(sv[0] * inv, sv[1] * inv), pk(sv[2] * inv, sv[3] * inv), pk(sv[4] * inv, sv[5] * inv), pk(sv[6] * inv, sv[7] * inv)};
    u32x4 pm = {pk(sv[8] * inv, sv[9] * inv), pk(sv[10] * inv, sv[11] * inv), 0u, 0u};
    #pragma unroll
    for (int ks = 0; ks < 4; ++ks) {
      f32x4 o0 = MFMA(BC8(vf[2 * ks]), BC8(pf), z), o1 = MFMA(BC8(vf[2 * ks + 1]), BC8(pf), z);
      u32x4 v0 = {vmf[2 * ks].x, vmf[2 * ks].y, 0u, 0u}, v1 = {vmf[2 * ks + 1].x, vmf[2 * ks + 1].y, 0u, 0u};
      o0 = MFMA(BC8(v0), BC8(pm), o0); o1 = MFMA(BC8(v1), BC8(pm), o1);
      u32x4 o = {pk(o0[0], o0[1]), pk(o0[2], o0[3]), pk(o1[0], o1[1]), pk(o1[2], o1[3])};
      ofx[(w * 4 + ks) * 64 + lane] = o;
    }
    __syncthreads();
    f32x4 acc[8][2];
    #pragma unroll
    for (int h2 = 0; h2 < 2; ++h2)
    #pragma unroll
    for (int ks = 0; ks < 4; ++ks) {
      bf16x8 b0 = BC8(ofx[((h2 * 2 + 0) * 4 + ks) * 64 + lane]), b1 = BC8(ofx[((h2 * 2 + 1) * 4 + ks) * 64 + lane]);
      #pragma unroll
      for (int c = 0; c < 8; ++c) {
        acc[c][0] = MFMA(wo[h2][c][ks], b0, (h2 | ks) ? acc[c][0] : z);
        acc[c][1] = MFMA(wo[h2][c][ks], b1, (h2 | ks) ? acc[c][1] : z);
      }
    }
    const unsigned opa = O_PA + (s * 16 + a) * 65536;
    #pragma unroll
    for (int c = 0; c < 8; ++c)
    #pragma unroll
    for (int rt = 0; rt < 2; ++rt) ST128(__builtin_bit_cast(u32x4, acc[c][rt]), (16 * rt + i) * 2048 + (128 * w + 16 * c + 4 * g) * 4, opa);
    DRAIN();
    __syncthreads();
    if (tid == 0) ST32(1u, a * 4, O_FPA + (l * 64 + s) * 128);
  }
}

__device__ __forceinline__ void role_ffn(const Params& p, rsrc_t ws, char* lds, const int l, const int f) {
  const int tid = threadIdx.x, lane = tid & 63, w = __builtin_amdgcn_readfirstlane(tid >> 6), i = lane & 15, g = lane >> 4;
  u32x4* lx = (u32x4*)lds;
  u32x4* hx = (u32x4*)(lds + 32768);
  bf16x8* lwd = (bf16x8*)(lds + 40960) + w * 1024 + lane;
  volatile int* okw = (volatile int*)(lds + 40960 + 65536);
  bf16x8 wg[2][2][16], wd[4][4];
  {
    const bf16x8* src = (const bf16x8*)(p.wsp + O_WGU) + (size_t)((f * 4 + w) * 64) * 64 + lane;
    #pragma unroll
    for (int m = 0; m < 2; ++m)
    #pragma unroll
    for (int t = 0; t < 2; ++t) {
      #pragma unroll
      for (int kk = 0; kk < 16; ++kk) wg[m][t][kk] = src[((m * 2 + t) * 16 + kk) * 64];
      SB();
      #pragma unroll
      for (int kk = 0; kk < 16; ++kk) { if ((m * 2 + t) * 16 + kk < 44) PINA(wg[m][t][kk]); }
      SB();
    }
    const bf16x8* sd = (const bf16x8*)(p.wsp + O_WD) + (size_t)(f * 32 + w * 8) * 256 + lane;
    #pragma unroll
    for (int c = 0; c < 4; ++c)
    #pragma unroll
    for (int ks = 0; ks < 4; ++ks) { wd[c][ks] = sd[(c * 4 + ks) * 64]; lwd[(c * 4 + ks) * 64] = sd[((c + 4) * 4 + ks) * 64]; }
  }
  const f32x4 z = {0, 0, 0, 0};
  for (int s = 0; s < 64; ++s) {
    if (w == 0) { bool ok = wait_flags(ws, O_FXN1 + (l * 64 + s) * 128, 32, lane); if (lane == 0) *okw = ok; }
    __syncthreads();
    if (!*okw) return;
    {
      u32x4 xr[8];
      #pragma unroll
      for (int k = 0; k < 8; ++k) xr[k] = LD128(tid * 16, O_XN1 + (l * 64 + s) * 32768 + k * 4096);
      stage_x(lx, xr, tid);
    }
    __syncthreads();
    f32x4 ag[2][2][2];
    #pragma unroll
    for (int kk = 0; kk < 16; ++kk) {
      const int c16 = kk * 4 + g;
      bf16x8 x0 = BC8(lx[c16 * 32 + (i ^ (c16 & 31))]), x1 = BC8(lx[c16 * 32 + ((i + 16) ^ (c16 & 31))]);
      #pragma unroll
      for (int m = 0; m < 2; ++m)
      #pragma unroll
      for (int t = 0; t < 2; ++t) {
        ag[m][t][0] = MFMA(wg[m][t][kk], x0, kk ? ag[m][t][0] : z);
        ag[m][t][1] = MFMA(wg[m][t][kk], x1, kk ? ag[m][t][1] : z);
      }
    }
    #pragma unroll
    for (int rt = 0; rt < 2; ++rt) {
      float hv[8];
      #pragma unroll
      for (int t = 0; t < 2; ++t)
      #pragma unroll
      for (int b = 0; b < 4; ++b) {
        float gv = rb(ag[0][t][rt][b]), uv = rb(ag[1][t][rt][b]);
        float inner = 0.7978845608028654f * (gv + 0.044715f * gv * gv * gv);
        float ex = __builtin_amdgcn_exp2f(inner * 2.8853900817779268f);
        float th = 1.0f - 2.0f * __builtin_amdgcn_rcpf(ex + 1.0f);
        hv[t * 4 + b] = rb(0.5f * gv * (1.0f + th)) * uv;
      }
      u32x4 o = {pk(hv[0], hv[1]), pk(hv[2], hv[3]), pk(hv[4], hv[5]), pk(hv[6], hv[7])};
      hx[(rt * 4 + w) * 64 + lane] = o;
    }
    __syncthreads();
    f32x4 acc[8][2];
    #pragma unroll
    for (int ks = 0; ks < 4; ++ks) {
      bf16x8 b0 = BC8(hx[ks * 64 + lane]), b1 = BC8(hx[(4 + ks) * 64 + lane]);
      #pragma unroll
      for (int c = 0; c < 8; ++c) {
        bf16x8 wv = c < 4 ? wd[c & 3][ks] : lwd[((c - 4) * 4 + ks) * 64];
        acc[c][0] = MFMA(wv, b0, ks ? acc[c][0] : z);
        acc[c][1] = MFMA(wv, b1, ks ? acc[c][1] : z);
      }
    }
    const unsigned opf = O_PF + (s * 14 + f) * 65536;
    #pragma unroll
    for (int c = 0; c < 8; ++c)
    #pragma unroll
    for (int rt = 0; rt < 2; ++rt) ST128(__builtin_bit_cast(u32x4, acc[c][rt]), (16 * rt + i) * 2048 + (128 * w + 16 * c + 4 * g) * 4, opf);
    DRAIN();
    __syncthreads();
    if (tid == 0) ST32(1u, f * 4, O_FPF + (l * 64 + s) * 128);
  }
}

__device__ __forceinline__ u32x4 ln_row(const float (&v)[8]) {
  float s = ((v[0] + v[1]) + (v[2] + v[3])) + ((v[4] + v[5]) + (v[6] + v[7]));
  const float mean = wave_sum(s) * (1.0f / 512.0f);
  float q = 0;
  #pragma unroll
  for (int j = 0; j < 8; ++j) { float d = v[j] - mean; q += d * d; }
  const float var = wave_sum(q) * (1.0f / 512.0f);
  const float mb = rb(mean), den = rb(sqrtf(rb(rb(var) + 1e-5f)));
  float y[8];
  #pragma unroll
  for (int j = 0; j < 8; ++j) y[j] = rb(v[j] - mb) / den;
  u32x4 o = {pk(y[0], y[1]), pk(y[2], y[3]), pk(y[4], y[5]), pk(y[6], y[7])};
  return o;
}
__device__ __forceinline__ void role_red(const Params& p, rsrc_t ws, char* lds, const int r) {
  const int tid = threadIdx.x, lane = tid & 63, w = __builtin_amdgcn_readfirstlane(tid >> 6);
  u32x4* xres = (u32x4*)lds + lane;
  if (p.role_mask & 32) {
    u32x4 xr[16];
    #pragma unroll
    for (int k = 0; k < 16; ++k) xr[k] = *(const u32x4*)(p.x + ((size_t)((k * 4 + w) * 32 + r) * 512 + lane * 8));
    #pragma unroll
    for (int k = 0; k < 16; ++k) {
      const int s = k * 4 + w;
      float v[8] = {lo16(xr[k].x), hi16(xr[k].x), lo16(xr[k].y), hi16(xr[k].y), lo16(xr[k].z), hi16(xr[k].z), lo16(xr[k].w), hi16(xr[k].w)};
      ST128(ln_row(v), lane * 16, O_XN + s * 32768 + r * 1024);
      DRAIN();
      if (lane == 0) ST32(1u, r * 4, O_FXN + s * 128);
    }
  }
  const int st = w & 1, l = w >> 1;
  if (!(p.layer_mask & (1 << l)) || !(p.role_mask & (st ? 16 : 8))) return;
  const int np = st ? 14 : 16;
  for (int s = 0; s < 64; ++s) {
    u32x4 xv;
    if (w == 0) xv = *(const u32x4*)(p.x + ((size_t)(s * 32 + r) * 512 + lane * 8));
    if (!wait_flags(ws, (st ? O_FPF : O_FPA) + (l * 64 + s) * 128, np, lane)) return;
    const unsigned ob = (st ? O_PF + s * 14 * 65536 : O_PA + s * 16 * 65536) + r * 2048;
    u32x4 a[16], b[16];
    #pragma unroll
    for (int k = 0; k < 16; ++k) { if (k < 14 || !st) { a[k] = LD128(lane * 32, ob + k * 65536); b[k] = LD128(lane * 32 + 16, ob + k * 65536); } }
    if (w > 0) xv = xres[s * 64];
    f32x4 sa = __builtin_bit_cast(f32x4, a[0]), sb = __builtin_bit_cast(f32x4, b[0]);
    #pragma unroll
    for (int k = 1; k < 16; ++k) { if (k < 14 || !st) { sa += __builtin_bit_cast(f32x4, a[k]); sb += __builtin_bit_cast(f32x4, b[k]); } }
    float v[8];
    v[0] = rb(lo16(xv.x) + rb(sa[0])); v[1] = rb(hi16(xv.x) + rb(sa[1])); v[2] = rb(lo16(xv.y) + rb(sa[2])); v[3] = rb(hi16(xv.y) + rb(sa[3]));
    v[4] = rb(lo16(xv.z) + rb(sb[0])); v[5] = rb(hi16(xv.z) + rb(sb[1])); v[6] = rb(lo16(xv.w) + rb(sb[2])); v[7] = rb(hi16(xv.w) + rb(sb[3]));
    u32x4 xo = {pk(v[0], v[1]), pk(v[2], v[3]), pk(v[4], v[5]), pk(v[6], v[7])};
    if (w == 3) {
      *(u32x4*)(p.out + ((size_t)(s * 32 + r) * 512 + lane * 8)) = xo;
    } else {
      xres[s * 64] = xo;
      const unsigned od = st ? O_XN + (64 + s) * 32768 : O_XN1 + (l * 64 + s) * 32768;
      ST128(ln_row(v), lane * 16, od + r * 1024);
      asm volatile("s_waitcnt vmcnt(0) lgkmcnt(0)" ::: "memory");
      if (lane == 0) ST32(1u, r * 4, st ? O_FXN + (64 + s) * 128 : O_FXN1 + (l * 64 + s) * 128);
    }
  }
}

__global__ void __launch_bounds__(256, 1) k_main(Params p) {
  extern __shared__ __attribute__((aligned(16))) char lds[];
  rsrc_t ws = __builtin_amdgcn_make_buffer_rsrc(p.wsp, 0, 256u * 1024 * 1024, 0x00020000);
  const int b = blockIdx.x;
  if (b < 64) { if ((p.role_mask & 1) && (p.layer_mask & (1 << (b >> 5)))) role_chain(p, ws, lds, b >> 5, b & 31); }
  else if (b < 160) { if (p.role_mask & 2) role_qkv(p, ws, lds, 2 - ((b - 64) >> 5), b & 31); }
  else if (b < 192) { if ((p.role_mask & 4) && (p.layer_mask & (1 << ((b - 160) >> 4)))) role_att(p, ws, lds, (b - 160) >> 4, b & 15); }
  else if (b < 220) { const int l = (b - 192) >= 14; if ((p.role_mask & 64) && (p.layer_mask & (1 << l))) role_ffn(p, ws, lds, l, b - 192 - 14 * l); }
  else if (b < 252) { if (p.role_mask & (8 | 16 | 32)) role_red(p, ws, lds, b - 220); }
}

extern "C" void kernel_launch(void* const* d_in, const int* in_sizes, int n_in, void* d_out, int out_size, void* d_ws, size_t ws_size, hipStream_t stream) {
  const u16* x = (const u16*)d_in[0]; const u16* mem0 = (const u16*)d_in[1];
  const u16* Wq = (const u16*)d_in[2]; const u16* Wk = (const u16*)d_in[3]; const u16* Wv = (const u16*)d_in[4]; const u16* Wo = (const u16*)d_in[5];
  const u16* wg = (const u16*)d_in[8]; const u16* wu = (const u16*)d_in[9]; const u16* wd = (const u16*)d_in[10];
  (void)hipFuncSetAttribute((const void*)k_main, hipFuncAttributeMaxDynamicSharedMemorySize, LDS_BYTES);
  (void)hipMemsetAsync(d_ws, 0xff, CTRL_BYTES, stream);
  const int NP = 32 * 4 * 3 * 2 * 16 * 64 + 32 * 32 * 4 * 64 + 14 * 4 * 2 * 2 * 16 * 64 + 14 * 32 * 4 * 64;
  k_prep<<<(NP + 255) / 256, 256, 0, stream>>>(Wq, Wk, Wv, Wo, wg, wu, wd, (char*)d_ws);
  Params p{x, mem0, (u16*)d_out, (char*)d_ws, 127, 3};
  k_main<<<256, 256, LDS_BYTES, stream>>>(p);
}
```

```cpp
#include <hip/hip_runtime.h>
#include <hip/hip_bf16.h>
#include <stdint.h>

typedef unsigned short u16;
typedef short bf16x8 __attribute__((ext_vector_type(8)));
typedef float f32x4 __attribute__((ext_vector_type(4)));
typedef float f32x2 __attribute__((ext_vector_type(2)));
typedef unsigned u32x4 __attribute__((ext_vector_type(4)));
typedef unsigned u32x2 __attribute__((ext_vector_type(2)));
typedef __bf16 bfx2 __attribute__((ext_vector_type(2)));

constexpr unsigned O_FXN  = 0;
constexpr unsigned O_FXN1 = O_FXN + 524288;
constexpr unsigned O_FPA  = O_FXN1 + 524288;
constexpr unsigned O_FPF  = O_FPA + 262144;
constexpr unsigned O_ABT  = O_FPF + 262144;
constexpr unsigned O_FH   = O_ABT + 256;
constexpr unsigned O_MEM  = O_FH + 1048576;
constexpr unsigned O_PC   = O_MEM + 1048576;
constexpr unsigned CTRL_BYTES = (O_PC + 1048576 + 16383) & ~16383u;
constexpr unsigned O_WQKV = (CTRL_BYTES + 4095) & ~4095u;
constexpr unsigned O_WO   = O_WQKV + 3 * 512 * 4096 * 2;
constexpr unsigned O_WGU  = O_WO + 4096 * 512 * 2;
constexpr unsigned O_WD   = O_WGU + 2 * 512 * 1792 * 2;
constexpr unsigned O_XN   = O_WD + 1792 * 512 * 2;
constexpr unsigned O_XN1  = O_XN + 2 * 64 * 32768;
constexpr unsigned O_Q    = O_XN1 + 2 * 64 * 32768;
constexpr unsigned QKV_SZ = 64 * 32 * 8192;
constexpr unsigned O_KM   = O_Q + 3 * QKV_SZ;
constexpr unsigned O_VMT  = O_KM + 2 * 64 * 32 * 2048;
constexpr unsigned O_PA   = O_VMT + 2 * 64 * 32 * 2048;
constexpr unsigned O_PF   = O_PA + 64u * 16 * 32768;
constexpr unsigned WS_END = O_PF + 64u * 14 * 32768;
static_assert(WS_END <= 256u * 1024 * 1024, "workspace");

constexpr int LDS_BYTES = 160 * 1024;
#define SC1 16
#define OOB 0x7fffff00u
#define EMPTY 0xffffffffu
#define SPIN_CAP (1u << 20)
#define DELAY_A 12
#define DELAY_B 16

#define POLL_SLEEP 4
#define SLEEPN(c, r) __builtin_amdgcn_s_sleep(c)
#define g_delay 0
#define SB() __builtin_amdgcn_sched_barrier(0)
#define MFMA(a, b, c) __builtin_amdgcn_mfma_f32_16x16x32_bf16(a, b, c, 0, 0, 0)
#define BC8(x) __builtin_bit_cast(bf16x8, x)
#define MA0(acc, A, B) asm volatile("v_mfma_f32_16x16x32_bf16 %0, %1, %2, 0" : "=v"(acc) : "a"(A), "v"(B))
#define MA1(acc, A, B) asm volatile("v_mfma_f32_16x16x32_bf16 %0, %1, %2, %0" : "+v"(acc) : "a"(A), "v"(B))
#define MV0(acc, A, B) asm volatile("v_mfma_f32_16x16x32_bf16 %0, %1, %2, 0" : "=v"(acc) : "v"(A), "v"(B))
#define MV1(acc, A, B) asm volatile("v_mfma_f32_16x16x32_bf16 %0, %1, %2, %0" : "+v"(acc) : "v"(A), "v"(B))
#define NOP(n) asm volatile("s_nop %0" :: "i"(n))
#define PINA(x) asm volatile("" : "+a"(x))
#define DPP8(x) ((unsigned)__builtin_amdgcn_update_dpp(0, (int)(x), 0x128, 0xf, 0xf, false))
#define DRAIN() asm volatile("s_waitcnt vmcnt(0)" ::: "memory")
#define MEMFENCE() asm volatile("" ::: "memory")
#define LBAR() do { asm volatile("s_waitcnt lgkmcnt(0)" ::: "memory"); __builtin_amdgcn_s_barrier(); asm volatile("" ::: "memory"); } while (0)
#define LD128(off, so) __builtin_amdgcn_raw_buffer_load_b128(ws, (off), (so), SC1)
#define LD64(off, so) __builtin_amdgcn_raw_buffer_load_b64(ws, (off), (so), SC1)
#define LD32(off, so) __builtin_amdgcn_raw_buffer_load_b32(ws, (off), (so), SC1)
#define ST128(v, off, so) __builtin_amdgcn_raw_buffer_store_b128((v), ws, (off), (so), SC1)
#define ST64(v, off, so) __builtin_amdgcn_raw_buffer_store_b64((v), ws, (off), (so), SC1)
#define ST32(v, off, so) __builtin_amdgcn_raw_buffer_store_b32((v), ws, (off), (so), SC1)
typedef __amdgpu_buffer_rsrc_t rsrc_t;

__device__ __forceinline__ unsigned pk(float a, float b) { f32x2 v = {a, b}; bfx2 r = __builtin_convertvector(v, bfx2); return __builtin_bit_cast(unsigned, r); }
__device__ __forceinline__ float rb(float a) { return __uint_as_float(pk(a, a) << 16); }
__device__ __forceinline__ float lo16(unsigned u) { return __uint_as_float(u << 16); }
__device__ __forceinline__ float hi16(unsigned u) { return __uint_as_float(u & 0xffff0000u); }
__device__ __forceinline__ float red_max(float x) {
  auto r = __builtin_amdgcn_permlane32_swap(__float_as_uint(x), __float_as_uint(x), false, false);
  x = fmaxf(__uint_as_float(r[0]), __uint_as_float(r[1]));
  auto q = __builtin_amdgcn_permlane16_swap(__float_as_uint(x), __float_as_uint(x), false, false);
  return fmaxf(__uint_as_float(q[0]), __uint_as_float(q[1]));
}
__device__ __forceinline__ float red_sum(float x) {
  auto r = __builtin_amdgcn_permlane32_swap(__float_as_uint(x), __float_as_uint(x), false, false);
  x = __uint_as_float(r[0]) + __uint_as_float(r[1]);
  auto q = __builtin_amdgcn_permlane16_swap(__float_as_uint(x), __float_as_uint(x), false, false);
  return __uint_as_float(q[0]) + __uint_as_float(q[1]);
}
__device__ __forceinline__ float wave_sum(float x) {
  x += __uint_as_float((unsigned)__builtin_amdgcn_update_dpp(0, (int)__float_as_uint(x), 0x121, 0xf, 0xf, false));
  x += __uint_as_float((unsigned)__builtin_amdgcn_update_dpp(0, (int)__float_as_uint(x), 0x122, 0xf, 0xf, false));
  x += __uint_as_float((unsigned)__builtin_amdgcn_update_dpp(0, (int)__float_as_uint(x), 0x124, 0xf, 0xf, false));
  x += __uint_as_float(DPP8(__float_as_uint(x)));
  return red_sum(x);
}

__device__ __forceinline__ bool wait_flags(rsrc_t ws, unsigned off, int n, unsigned stride, int lane) {
  unsigned vo = lane < n ? (unsigned)lane * stride : OOB;
  for (unsigned spins = 0; spins < SPIN_CAP; ++spins) {
    MEMFENCE();
    unsigned v = LD32(vo, off);
    unsigned long long miss = __ballot(v == EMPTY);
    if (!miss) return true;
    const int fm = __builtin_ctzll(miss);
    const unsigned v1 = lane == fm ? vo : OOB;
    if (v != EMPTY) vo = OOB;
    for (; spins < SPIN_CAP; ++spins) {
      __builtin_amdgcn_s_sleep(POLL_SLEEP);
      MEMFENCE();
      if (__all(LD32(v1, off) != EMPTY)) break;
      if ((spins & 63u) == 63u) { if (__builtin_amdgcn_readfirstlane(LD32(0u, O_ABT)) == 1u) return false; }
    }
    if (lane == fm) vo = OOB;
  }
  if (lane == 0) ST32(1u, 0u, O_ABT);
  return false;
}

__device__ __forceinline__ void signal_cu(rsrc_t ws, unsigned* cnt, unsigned off, int lane) {
  if (lane == 0) { unsigned old = atomicAdd(cnt, 1u); if ((old & 3u) == 3u) ST32(1u, 0u, off); }
}
#define ST(k)
#define SK(k)
#define TR(ev, l, s, cond)
struct Params {
  const u16* x; const u16* mem0; u16* out; char* wsp; int role_mask; int layer_mask;
};

constexpr int PB1 = 3 * 16 * 8, PB2 = 128, PB3 = 2 * 16 * 4, PB4 = 56;
constexpr int PREP_B = PB1 + PB2 + PB3 + PB4, INIT_B = CTRL_BYTES / 16384;
__global__ void __launch_bounds__(256) k_prep(const u16* Wq, const u16* Wk, const u16* Wv, const u16* Wo, const u16* wg, const u16* wu, const u16* wd, char* wsp) {
  __shared__ u32x4 t[2048];
  int b = blockIdx.x; const int tid = threadIdx.x;
  if (b >= PREP_B) {
    const u32x4 e = {EMPTY, EMPTY, EMPTY, EMPTY};
    #pragma unroll
    for (int k = 0; k < 4; ++k) ((u32x4*)wsp)[(b - PREP_B) * 1024 + k * 256 + tid] = e;
    return;
  }
  const u16* src; int ld, kk, np, nwin = 16; char* dst; int wstride, tstride;
  if (b < PB1) {
    const int m = b >> 7, id = b & 127; kk = id >> 3; np = id & 7;
    src = m == 0 ? Wq : (m == 1 ? Wk : Wv); ld = 4096;
    dst = wsp + O_WQKV + (size_t)((np * 16 * 3 + m) * 32 + kk) * 1024; wstride = 3 * 32768; tstride = 16384;
  } else if (b < PB1 + PB2) {
    kk = b - PB1; np = 0; src = Wo; ld = 512;
    dst = wsp + O_WO + (size_t)((kk >> 2) * 128 + (kk & 3)) * 1024; wstride = 8192; tstride = 4096;
  } else if (b < PB1 + PB2 + PB3) {
    const int id = b - PB1 - PB2; const int m = id >> 6; kk = (id >> 2) & 15; np = id & 3;
    src = m == 0 ? wg : wu; ld = 1792; if (np == 3) nwin = 8;
    dst = wsp + O_WGU + (size_t)((np * 16 * 2 + m) * 32 + kk) * 1024; wstride = 2 * 32768; tstride = 16384;
  } else {
    kk = b - PB1 - PB2 - PB3; np = 0; src = wd; ld = 512;
    dst = wsp + O_WD + (size_t)((kk >> 2) * 128 + (kk & 3)) * 1024; wstride = 8192; tstride = 4096;
  }
  const int g = tid >> 6, n8 = tid & 63;
  if ((n8 >> 2) < nwin) {
    u32x4 r[8];
    #pragma unroll
    for (int j = 0; j < 8; ++j) r[j] = *(const u32x4*)(src + (size_t)(kk * 32 + g * 8 + j) * ld + np * 512 + n8 * 8);
    #pragma unroll
    for (int c = 0; c < 8; ++c) {
      u32x4 o;
      #pragma unroll
      for (int q = 0; q < 4; ++q) {
        const unsigned lo = r[2 * q][c >> 1], hi = r[2 * q + 1][c >> 1];
        o[q] = (c & 1) ? ((lo >> 16) | (hi & 0xffff0000u)) : ((lo & 0xffffu) | (hi << 16));
      }
      t[((n8 >> 2) * 2 + (c >> 2)) * 64 + g * 16 + (n8 & 3) * 4 + (c & 3)] = o;
    }
  }
  __syncthreads();
  #pragma unroll
  for (int k = 0; k < 8; ++k) {
    const int f = k * 4 + g;
    if ((f >> 1) < nwin) *(u32x4*)(dst + (size_t)(f >> 1) * wstride + (f & 1) * tstride + n8 * 16) = t[f * 64 + n8];
  }
}

__device__ __forceinline__ void role_chain(const Params& p, rsrc_t ws, char* lds, const int l, const int h) {
  const int tid = threadIdx.x, lane = tid & 63, w = __builtin_amdgcn_readfirstlane(tid >> 6), i = lane & 15, g = lane >> 4;
  const int vi = g * 8 + (i & 7);
  bf16x8* ldsWo = (bf16x8*)lds;
  bf16x8* memf = (bf16x8*)(lds + 131072);
  f32x4* sc = (f32x4*)(lds + 131072 + 8192);
  bf16x8* of = (bf16x8*)(lds + 131072 + 8192 + 7168);
  bf16x8* wx = (bf16x8*)(lds + 131072 + 8192 + 7168 + 2048) + w * 192 + lane;
  unsigned* cnt = (unsigned*)(lds + 131072 + 8192 + 7168 + 2048 + 12288);
  bf16x8 wf[3][2][16];
  {
    const bf16x8* src = (const bf16x8*)(p.wsp + O_WQKV) + (size_t)((h * 4 + w) * 96) * 64 + lane;
    #pragma unroll
    for (int m = 0; m < 3; ++m)
    #pragma unroll
    for (int t = 0; t < 2; ++t) {
      #pragma unroll
      for (int kk = 0; kk < 16; ++kk) { wf[m][t][kk] = src[((m * 2 + t) * 16 + kk) * 64]; }
      if (m == 2) { wx[(t * 2 + 0) * 64] = wf[m][t][14]; if (t == 0) wx[64] = wf[m][t][15]; }
      SB();
      #pragma unroll
      for (int kk = 0; kk < 16; ++kk) { if (m < 2) PINA(wf[m][t][kk]); }
      SB();
    }
    const bf16x8* so = (const bf16x8*)(p.wsp + O_WO) + (size_t)h * 8192;
    for (int k = 0; k < 32; ++k) ldsWo[k * 256 + tid] = so[k * 256 + tid];
  }
  rsrc_t rM0 = __builtin_amdgcn_make_buffer_rsrc((void*)p.mem0, 0, 8192, 0x00020000);
  const int prm = 8 * (i >> 2) + (i & 3);
  const int kOff = w * 1024 + lane * 16;
  const int vOff = w * 2048 + lane * 16;
  const unsigned oK = O_Q + QKV_SZ + h * 8192, oVT = O_Q + 2 * QKV_SZ + h * 8192;
  const unsigned oFH = O_FH + (l * 64 * 32 + h) * 256;
  const unsigned oKM = O_KM + (l * 64 * 32 + h) * 2048, oVMT = O_VMT + (l * 64 * 32 + h) * 2048;
  const unsigned oMEM = O_MEM + l * 64 * 8192, oPC = O_PC + l * 32 * 32 * 512;
  u32x4 kx[2], vx[2];
  if (tid == 0) *cnt = 0u;
  if (!wait_flags(ws, oFH + 4, 2, 4, lane)) return;
  #pragma unroll
  for (int t = 0; t < 2; ++t) { kx[t] = LD128(kOff, oK + t * 4096); vx[t] = LD128(vOff + t * 1024, oVT); }
  __syncthreads();
  const int hi = i >> 3;
  const bf16x8* mfp = memf + g * 8 + (i & 7);
  const bf16x8* wop = ldsWo + w * 2048 + lane;
  const f32x4* scx = sc + hi * 32 + vi;
  const f32x2* scm = (const f32x2*)(sc + 64 + vi) + hi;
  f32x4* scw = sc + w * 96 + vi;
  u32x4* ofw = hi ? (u32x4*)(sc) + 384 + lane : (u32x4*)of + w * 32 + vi;
  const bf16x8* ofr_p = of + vi;
  const int tid16 = tid * 16;
  const unsigned pcwOff = vi * 16 + hi * 16384;
  const unsigned kmOff = hi ? OOB : (unsigned)((w * 32 + vi) * 16);
  const unsigned vmOff = g < 2 ? (unsigned)(w * 512 + lane * 8) : OOB;
  const int pcrOff = (lane >> 3) * 2048 + (w * 8 + (lane & 7)) * 16;
  const unsigned memOff = lane < 8 ? (unsigned)(w * 128 + lane * 16) : OOB;
  const unsigned flOff = lane < 2 ? (unsigned)(lane * 4 + 4) : OOB;
  const float cexp = 0.08838834764831845f * 1.4426950408889634f;
  for (int s = 0; s < 64; ++s) {
    {
      u32x4 x[2];
      if (s == 0) {
        #pragma unroll
        for (int k = 0; k < 2; ++k) { int c = k * 256 + tid; x[k] = __builtin_amdgcn_raw_buffer_load_b128(rM0, ((c & 7) * 512 + (c >> 3) * 8) * 2, 0, 0); }
      } else {
        SLEEPN(DELAY_B, (p.role_mask >> 16) & 255);
        unsigned spins = 0;
        for (; spins < SPIN_CAP; ++spins) {
          MEMFENCE();
          #pragma unroll
          for (int k = 0; k < 2; ++k) x[k] = LD128(tid16, oMEM + s * 8192 + k * 4096);
          bool ok = true;
          #pragma unroll
          for (int k = 0; k < 2; ++k) ok &= (x[k].x != EMPTY) && (x[k].y != EMPTY) && (x[k].z != EMPTY) && (x[k].w != EMPTY);
          if (__all(ok)) break;
        }
        if (spins >= SPIN_CAP && lane == 0) ST32(1u, 0u, O_ABT);
      }
      ST(0); SK(0);
      #pragma unroll
      for (int k = 0; k < 2; ++k) ((u32x4*)memf)[tid + k * 256] = x[k];
    }
    LBAR();
    TR(0, l, s, tid == 0 && h == 0);
    ST(1);
    f32x4 aq[2], ak[2], av[2];
    bf16x8 mf[8];
    #pragma unroll
    for (int kk = 0; kk < 8; ++kk) mf[kk] = mfp[kk * 32];
    SB();
#define QK1(acc, m, kk, t) do { if ((kk) == 0) MA0(acc[t], wf[m][t][kk], mf[(kk) & 7]); else MA1(acc[t], wf[m][t][kk], mf[(kk) & 7]); SB(); } while (0)
#define V1(kk, t) do { if ((kk) == 0) MV0(av[t], mf[(kk) & 7], wf[2][t][kk]); else MV1(av[t], mf[(kk) & 7], wf[2][t][kk]); SB(); } while (0)
#define RL(kk) do { mf[(kk) & 7] = mfp[(((kk) + 8) & 15) * 32]; SB(); } while (0)
#define K2(kk) do { QK1(ak, 1, kk, 0); QK1(ak, 1, kk, 1); RL(kk); } while (0)
    #pragma unroll
    for (int kk = 0; kk < 16; ++kk) { QK1(aq, 0, kk, 0); QK1(aq, 0, kk, 1); RL(kk); }
    ST(2);
    K2(0); K2(1); K2(2);
    u32x4 qf = {pk(aq[0][0], aq[0][1]), pk(aq[0][2], aq[0][3]), pk(aq[1][0], aq[1][1]), pk(aq[1][2], aq[1][3])}; SB();
    QK1(ak, 1, 3, 0); QK1(ak, 1, 3, 1);
    f32x4 s0, s1, s2;
    MV0(s0, kx[0], qf); SB();
    MV0(s1, kx[1], qf); SB();
    RL(3); K2(4); K2(5); QK1(ak, 1, 6, 0);
    scw[0] = s0; SB();
    QK1(ak, 1, 6, 1);
    scw[32] = s1; SB();
    QK1(ak, 1, 7, 0); QK1(ak, 1, 7, 1);
    LBAR();
    f32x4 xa = scx[0], xb = scx[96], xc = scx[192], xd = scx[288];
    SB();
    RL(6); RL(7);
    K2(8);
    unsigned nfl = LD32(flOff, oFH + ((s + 1) & 63) * 8192); SB();
    K2(9);
    xa = xa + xb; SB();
    QK1(ak, 1, 10, 0);
    xc = xc + xd; SB();
    QK1(ak, 1, 10, 1); RL(10);
    xa = xa + xc; SB();
    QK1(ak, 1, 11, 0);
    float mx = fmaxf(fmaxf(xa[0], xa[1]), fmaxf(xa[2], xa[3])); SB();
    QK1(ak, 1, 11, 1); RL(11);
    mx = fmaxf(mx, __uint_as_float(DPP8(__float_as_uint(mx)))); SB();
    QK1(ak, 1, 12, 0);
    { auto r = __builtin_amdgcn_permlane32_swap(__float_as_uint(mx), __float_as_uint(mx), false, false); mx = fmaxf(__uint_as_float(r[0]), __uint_as_float(r[1])); } SB();
    QK1(ak, 1, 12, 1); RL(12);
    { auto r = __builtin_amdgcn_permlane16_swap(__float_as_uint(mx), __float_as_uint(mx), false, false); mx = fmaxf(__uint_as_float(r[0]), __uint_as_float(r[1])); }
    float nm = -mx * cexp; SB();
    QK1(ak, 1, 13, 0);
    float e0 = __builtin_amdgcn_exp2f(fmaf(xa[0], cexp, nm)), e1 = __builtin_amdgcn_exp2f(fmaf(xa[1], cexp, nm)); SB();
    QK1(ak, 1, 13, 1); RL(13);
    float e2 = __builtin_amdgcn_exp2f(fmaf(xa[2], cexp, nm)), e3 = __builtin_amdgcn_exp2f(fmaf(xa[3], cexp, nm)); SB();
    QK1(ak, 1, 14, 0);
    unsigned p0 = pk(e0, e1), p1 = pk(e2, e3); SB();
    QK1(ak, 1, 14, 1); RL(14);
    u32x4 pf = {p0, p1, DPP8(p0), DPP8(p1)}; SB();
    QK1(ak, 1, 15, 0);
    float lx = (lo16(p0) + hi16(p0)) + (lo16(p1) + hi16(p1)); SB();
    QK1(ak, 1, 15, 1); RL(15);
    ST(3);
    f32x4 ao[2];
    MV0(ao[0], vx[0], pf); SB();
    MV0(ao[1], vx[1], pf); SB();
    V1(0, 0); V1(0, 1); RL(0); V1(1, 0); V1(1, 1); RL(1);
    u32x4 kf = {pk(ak[0][0], ak[0][1]), pk(ak[0][2], ak[0][3]), pk(ak[1][0], ak[1][1]), pk(ak[1][2], ak[1][3])}; SB();
    V1(2, 0); V1(2, 1);
    MV0(s2, kf, qf); SB();
    RL(2); V1(3, 0);
    ST128(kf, kmOff, oKM + s * 65536); SB();
    V1(3, 1); RL(3); V1(4, 0); V1(4, 1); RL(4); V1(5, 0);
    scw[64] = s2; SB();
    V1(5, 1); V1(6, 0); V1(6, 1);
    LBAR();
    f32x2 ma = scm[0], mb = scm[192], mc = scm[384], md = scm[576];
    SB();
    RL(5); RL(6);
    V1(7, 0); V1(7, 1); RL(7);
    V1(8, 0);
    wf[2][0][14] = wx[0]; wf[2][1][14] = wx[128]; wf[2][0][15] = wx[64]; SB();
    V1(8, 1);
    ma = ma + mb; mc = mc + md; SB();
    V1(9, 0);
    ma = ma + mc;
    if (g >= 2) { ma[0] = -INFINITY; ma[1] = -INFINITY; }
    SB();
    V1(9, 1);
    float mm = fmaxf(fmaxf(ma[0], ma[1]), mx); SB();
    V1(10, 0);
    mm = fmaxf(mm, __uint_as_float(DPP8(__float_as_uint(mm)))); SB();
    V1(10, 1);
    { auto r = __builtin_amdgcn_permlane32_swap(__float_as_uint(mm), __float_as_uint(mm), false, false); mm = fmaxf(__uint_as_float(r[0]), __uint_as_float(r[1])); } SB();
    V1(11, 0);
    { auto r = __builtin_amdgcn_permlane16_swap(__float_as_uint(mm), __float_as_uint(mm), false, false); mm = fmaxf(__uint_as_float(r[0]), __uint_as_float(r[1])); }
    float nm2 = -mm * cexp; SB();
    V1(11, 1);
    float e4 = __builtin_amdgcn_exp2f(fmaf(ma[0], cexp, nm2)), e5 = __builtin_amdgcn_exp2f(fmaf(ma[1], cexp, nm2)); SB();
    V1(12, 0);
    float fx = __builtin_amdgcn_exp2f(fmaf(mx, cexp, nm2));
    unsigned p2 = pk(e4, e5); SB();
    V1(12, 1);
    float sum = fmaf(lx, fx, lo16(p2) + hi16(p2));
    u32x4 pm4 = {p2, DPP8(p2), 0u, 0u}; SB();
    V1(13, 0);
    sum += __uint_as_float(DPP8(__float_as_uint(sum)));
    ao[0] *= fx; SB();
    V1(13, 1);
    { auto r = __builtin_amdgcn_permlane32_swap(__float_as_uint(sum), __float_as_uint(sum), false, false); sum = __uint_as_float(r[0]) + __uint_as_float(r[1]); }
    ao[1] *= fx; SB();
    V1(14, 0);
    { auto r = __builtin_amdgcn_permlane16_swap(__float_as_uint(sum), __float_as_uint(sum), false, false); sum = __uint_as_float(r[0]) + __uint_as_float(r[1]); } SB();
    V1(14, 1);
    float inv = __builtin_amdgcn_rcpf(sum); SB();
    V1(15, 0); V1(15, 1);
    ST(4);
    bf16x8 wo[16];
    #pragma unroll
    for (int k = 0; k < 8; ++k) wo[k] = wop[k * 64];
    SB();
    u32x4 vm0 = {pk(av[0][0], av[0][1]), pk(av[0][2], av[0][3]), 0u, 0u}; SB();
    u32x4 vm1 = {pk(av[1][0], av[1][1]), pk(av[1][2], av[1][3]), 0u, 0u}; SB();
    MV1(ao[0], vm0, pm4); SB();
    NOP(0); SB();
    MV1(ao[1], vm1, pm4); SB();
    #pragma unroll
    for (int k = 8; k < 16; ++k) wo[k] = wop[k * 64];
    SB();
    u32x4 ofr = {pk(ao[0][0] * inv, ao[0][1] * inv), pk(ao[0][2] * inv, ao[0][3] * inv), pk(ao[1][0] * inv, ao[1][1] * inv), pk(ao[1][2] * inv, ao[1][3] * inv)};
    *ofw = ofr;
    SB();
    { u32x2 t0 = {vm0.x, vm0.y}, t1 = {vm1.x, vm1.y};
      ST64(t0, vmOff, oVMT + s * 65536);
      ST64(t1, vmOff + 256, oVMT + s * 65536); }
    if (s == 63) break;
    SB();
    LBAR();
    ST(5);
    bf16x8 ob[4];
    #pragma unroll
    for (int ks = 0; ks < 4; ++ks) ob[ks] = ofr_p[ks * 32];
    SB();
    const unsigned tag = s & 1;
    f32x4 accA[2], accB[2];
    #pragma unroll
    for (int pr = 0; pr < 5; ++pr) {
      u32x4 o;
      #pragma unroll
      for (int ks = 0; ks < 4; ++ks) {
        if (pr < 4) { if (ks == 0) MV0(accA[pr & 1], wo[(pr & 1) * 8 + ks], ob[ks]); else MV1(accA[pr & 1], wo[(pr & 1) * 8 + ks], ob[ks]); SB(); }
        if (pr < 4) { if (ks == 0) MV0(accB[pr & 1], wo[(pr & 1) * 8 + 4 + ks], ob[ks]); else MV1(accB[pr & 1], wo[(pr & 1) * 8 + 4 + ks], ob[ks]); SB(); }
        if (pr == 4 && ks == 0) { NOP(7); SB(); }
        if (pr > 0 && (ks > 1 || pr == 4)) {
          #pragma unroll
          for (int e = (pr == 4 ? ks : (ks - 2) * 2); e < (pr == 4 ? ks + 1 : (ks - 2) * 2 + 2); ++e) { float v = hi ? accB[(pr - 1) & 1][e] : accA[(pr - 1) & 1][e]; o[e] = (__float_as_uint(v) & ~1u) | tag; }
          SB(); }
        if (pr < 2) { wo[(pr & 1) * 8 + ks] = wop[((pr + 2) * 8 + ks) * 64]; wo[(pr & 1) * 8 + 4 + ks] = wop[((pr + 2) * 8 + 4 + ks) * 64]; SB(); }
      }
      if (pr > 0) { ST128(o, pcwOff, oPC + ((w * 8 + 2 * (pr - 1)) * 32 + h) * 512); SB(); }
    }
    ST(6); SK(1);
    if (!__all(nfl != EMPTY)) { if (!wait_flags(ws, oFH + (s + 1) * 8192 + 4, 2, 4, lane)) {   } }
    #pragma unroll
    for (int t = 0; t < 2; ++t) { kx[t] = LD128(kOff, oK + (s + 1) * 262144 + t * 4096); vx[t] = LD128(vOff + t * 1024, oVT + (s + 1) * 262144); }
    SB();
    asm volatile("s_waitcnt vmcnt(8)" ::: "memory");
    signal_cu(ws, cnt, oFH + s * 8192 + 128, lane);
    {
      ST(7);
      SLEEPN(DELAY_A, (p.role_mask >> 8) & 255);
      u32x4 x[4];
      unsigned spins = 0;
      for (; spins < SPIN_CAP; ++spins) {
        MEMFENCE();
        #pragma unroll
        for (int k = 0; k < 4; ++k) x[k] = LD128(pcrOff + k * 512, oPC + h * 16384);
        unsigned an = 1, orr = 0;
        #pragma unroll
        for (int k = 0; k < 4; ++k) { an &= x[k].x & x[k].y & x[k].z & x[k].w; orr |= x[k].x | x[k].y | x[k].z | x[k].w; }
        bool ok = tag ? (an & 1) : !(orr & 1);
        if (__all(ok)) break;
      }
      if (spins >= SPIN_CAP && lane == 0) ST32(1u, 0u, O_ABT);
      DRAIN();
      ST(8); SK(2);
      f32x4 a = (__builtin_bit_cast(f32x4, x[0]) + __builtin_bit_cast(f32x4, x[1])) + (__builtin_bit_cast(f32x4, x[2]) + __builtin_bit_cast(f32x4, x[3]));
      #pragma unroll
      for (int e = 0; e < 4; ++e) {
        float v = a[e];
        v += __uint_as_float(DPP8(__float_as_uint(v)));
        a[e] = red_sum(v);
      }
      u32x2 o = {pk(a[0], a[1]), pk(a[2], a[3])};
      if (o.x == EMPTY) o.x = 0xfffefffeu;
      if (o.y == EMPTY) o.y = 0xfffefffeu;
      ST64(o, memOff, oMEM + (s + 1) * 8192 + (h >> 1) * 512 + (h & 1) * 8);
      ST(9); SK(3);
    }
  }
  DRAIN();
  signal_cu(ws, cnt, oFH + 63 * 8192 + 128, lane);
#undef V1
#undef RL
#undef QK1
#undef K2
}

__device__ __forceinline__ void stage_x(char* lx, const u32x4 (&xr)[8], int tid) {
  char* b = lx + (tid & 63) * 528 + (tid >> 6) * 16;
  #pragma unroll
  for (int k = 0; k < 8; ++k) *(u32x4*)(b + k * 64) = xr[k];
}
#define XFRAG(base, kk, rt, tokS) (*(const bf16x8*)((base) + (kk) * 2112 + (rt) * (tokS) * 16))

__device__ __forceinline__ void store_partial(rsrc_t ws, char* tp, const f32x4 (&acc)[8][2], unsigned obase, int w, int lane) {
  const int i = lane & 15, g = lane >> 4;
  char* wp = tp + i * 272 + g * 16;
  const char* rp = tp + g * 272 + i * 16;
  const unsigned so = g * 1024 + w * 256 + i * 16;
  #pragma unroll
  for (int rt = 0; rt < 2; ++rt)
  #pragma unroll
  for (int c = 0; c < 4; ++c) {
    u32x4 o = {pk(acc[2 * c][rt][0], acc[2 * c][rt][1]), pk(acc[2 * c][rt][2], acc[2 * c][rt][3]), pk(acc[2 * c + 1][rt][0], acc[2 * c + 1][rt][1]), pk(acc[2 * c + 1][rt][2], acc[2 * c + 1][rt][3])};
    *(u32x4*)(wp + rt * 4352 + c * 64) = o;
  }
  u32x4 v[8];
  #pragma unroll
  for (int k = 0; k < 8; ++k) v[k] = *(const u32x4*)(rp + k * 1088);
  #pragma unroll
  for (int k = 0; k < 8; ++k) ST128(v[k], so, obase + k * 4096);
}

template <int MM>
__device__ __forceinline__ void role_qkv(const Params& p, rsrc_t ws, char* lds, const int m, const int h) {
  const int tid = threadIdx.x, lane = tid & 63, w = __builtin_amdgcn_readfirstlane(tid >> 6), i = lane & 15, g = lane >> 4;
  int* dec = (int*)(lds + 34816);
  bf16x8 wf[2][16];
  {
    const bf16x8* src = (const bf16x8*)(p.wsp + O_WQKV) + (size_t)(((h * 4 + w) * 3 + m) * 32) * 64 + lane;
    #pragma unroll
    for (int t = 0; t < 2; ++t)
    #pragma unroll
    for (int kk = 0; kk < 16; ++kk) wf[t][kk] = src[(t * 16 + kk) * 64];
  }
  const int prm = 8 * (i >> 2) + (i & 3);
  constexpr bool ISV = MM == 2;
  constexpr int tokS = MM ? 4 : 16;
  const char* xb = lds + g * 528 + (MM ? prm : i) * 16;
  const unsigned stOff = ISV ? (unsigned)(w * 2048 + lane * 16) : (unsigned)(w * 1024 + lane * 16);
  int n0 = (p.layer_mask & 1) ? 0 : 64, n1 = (p.layer_mask & 2) ? 0 : 64;
  const unsigned flv = lane < 32 ? (unsigned)lane * 128u : OOB;
  u32x4 xr[8];
  int cur = -1;
  auto pick_blocking = [&]() -> int {
    if (w == 0) {
      int d = -2;
      if (n0 >= 64) { if (wait_flags(ws, O_FXN + (64 + n1) * 4096, 32, 128, lane)) d = 64 + n1; }
      else for (unsigned spins = 0; spins < SPIN_CAP; ++spins) {
        MEMFENCE();
        unsigned f1 = n1 < 64 ? LD32(flv, O_FXN + (64 + n1) * 4096) : EMPTY;
        unsigned f0 = n0 < 64 ? LD32(flv, O_FXN + n0 * 4096) : EMPTY;
        if (__all(f1 != EMPTY)) { d = 64 + n1; break; }
        if (__all(f0 != EMPTY)) { d = n0; break; }
        __builtin_amdgcn_s_sleep(POLL_SLEEP);
        if ((spins & 63u) == 63u) { if (__builtin_amdgcn_readfirstlane(LD32(0u, O_ABT)) == 1u) break; }
      }
      if (d == -2 && lane == 0) ST32(1u, 0u, O_ABT);
      if (lane == 0) *dec = d;
    }
    LBAR();
    int d = __builtin_amdgcn_readfirstlane(*dec);
    LBAR();
    return d;
  };
  auto issue = [&](int item) {
    #pragma unroll
    for (int k = 0; k < 8; ++k) xr[k] = LD128(tid * 16, O_XN + item * 32768 + k * 4096);
    if (item >= 64) ++n1; else ++n0;
  };
  if (n0 >= 64 && n1 >= 64) return;
  cur = pick_blocking();
  if (cur < 0) return;
  issue(cur);
  const f32x4 z = {0, 0, 0, 0};
  for (;;) {
    unsigned f1 = EMPTY, f0 = EMPTY;
    if (w == 0) {
      if (n1 < 64) f1 = LD32(flv, O_FXN + (64 + n1) * 4096);
      if (n0 < 64) f0 = LD32(flv, O_FXN + n0 * 4096);
    }
    stage_x(lds, xr, tid);
    if (w == 0) { int d = __all(f1 != EMPTY) ? 64 + n1 : (__all(f0 != EMPTY) ? n0 : -1); if (lane == 0) *dec = d; }
    LBAR();
    int nxt = __builtin_amdgcn_readfirstlane(*dec);
    bf16x8 xf[4][2];
    #pragma unroll
    for (int kk = 0; kk < 4; ++kk) { xf[kk][0] = XFRAG(xb, kk, 0, tokS); xf[kk][1] = XFRAG(xb, kk, 1, tokS); }
    SB();
    if (nxt >= 0) issue(nxt);
    SB();
    f32x4 acc[2][2];
    #pragma unroll
    for (int kk = 0; kk < 16; ++kk) {
      #pragma unroll
      for (int t = 0; t < 2; ++t)
      #pragma unroll
      for (int rt = 0; rt < 2; ++rt) {
        if (ISV) acc[t][rt] = MFMA(xf[kk & 3][rt], wf[t][kk], kk ? acc[t][rt] : z);
        else acc[t][rt] = MFMA(wf[t][kk], xf[kk & 3][rt], kk ? acc[t][rt] : z);
      }
      SB();
      if (kk + 4 < 16) { xf[kk & 3][0] = XFRAG(xb, kk + 4, 0, tokS); xf[kk & 3][1] = XFRAG(xb, kk + 4, 1, tokS); }
      SB();
    }
    const int s = cur & 63, l = cur >> 6;
    const unsigned ob = O_Q + m * QKV_SZ + (s * 32 + h) * 8192;
    if (ISV) {
      #pragma unroll
      for (int t = 0; t < 2; ++t) {
        u32x4 o = {pk(acc[t][0][0], acc[t][0][1]), pk(acc[t][0][2], acc[t][0][3]), pk(acc[t][1][0], acc[t][1][1]), pk(acc[t][1][2], acc[t][1][3])};
        ST128(o, stOff + t * 1024, ob);
      }
    } else {
      #pragma unroll
      for (int rt = 0; rt < 2; ++rt) {
        u32x4 o = {pk(acc[0][rt][0], acc[0][rt][1]), pk(acc[0][rt][2], acc[0][rt][3]), pk(acc[1][rt][0], acc[1][rt][1]), pk(acc[1][rt][2], acc[1][rt][3])};
        ST128(o, stOff, ob + rt * 4096);
      }
    }
    DRAIN();
    LBAR();
    if (tid == 0) ST32(1u, m * 4, O_FH + ((l * 64 + s) * 32 + h) * 256);
    TR(2, l, s, tid == 0 && h == 0 && MM == 1);
    if (nxt < 0) {
      if (n0 >= 64 && n1 >= 64) break;
      nxt = pick_blocking();
      if (nxt < 0) break;
      issue(nxt);
    }
    cur = nxt;
  }
}

__device__ __forceinline__ void role_att(const Params& p, rsrc_t ws, char* lds, const int l, const int a) {
  const int tid = threadIdx.x, lane = tid & 63, w = __builtin_amdgcn_readfirstlane(tid >> 6), i = lane & 15, g = lane >> 4;
  u32x4* ofx = (u32x4*)lds + lane;
  bf16x8* lwo = (bf16x8*)(lds + 32768 + 4 * 8704) + w * 1024 + lane;
  bf16x8 wo[2][8][4];
  #pragma unroll
  for (int hh = 0; hh < 2; ++hh) {
    const bf16x8* src = (const bf16x8*)(p.wsp + O_WO) + (size_t)((2 * a + hh) * 32 + w * 8) * 256 + lane;
    #pragma unroll
    for (int c = 0; c < 8; ++c)
    #pragma unroll
    for (int ks = 0; ks < 4; ++ks) { wo[hh][c][ks] = src[(c * 4 + ks) * 64]; if (hh == 1 && c >= 4) lwo[((c - 4) * 4 + ks) * 64] = wo[hh][c][ks]; }
    SB();
    #pragma unroll
    for (int c = 0; c < 8; ++c)
    #pragma unroll
    for (int ks = 0; ks < 4; ++ks) { if (hh * 32 + c * 4 + ks < 44) PINA(wo[hh][c][ks]); }
    SB();
  }
  const int hh = w >> 1, qh = w & 1, h = 2 * a + hh;
  const int prm = 8 * (i >> 2) + (i & 3);
  const int tq = 16 * qh + i;
  const float cexp = 0.08838834764831845f * 1.4426950408889634f;
  const f32x4 z = {0, 0, 0, 0};
  const unsigned l16 = lane * 16, kmOff = (g * 8 + (i & 7)) * 16, vmOff = (lane & 31) * 8;
  const unsigned oFH = O_FH + (l * 64 * 32 + h) * 256;
  unsigned* cnt = (unsigned*)(lds + 32768 + 4 * 8704 + 65536);
  if (tid == 0) *cnt = 0u;
  u32x4 qf[4], kf[2][4], vf[8];
  auto issue = [&](int s) {
    const unsigned oq = O_Q + (s * 32 + h) * 8192;
    #pragma unroll
    for (int ks = 0; ks < 4; ++ks) {
      qf[ks] = LD128(l16 + ks * 1024, oq + qh * 4096);
      kf[0][ks] = LD128(l16 + ks * 1024, oq + QKV_SZ);
      kf[1][ks] = LD128(l16 + ks * 1024, oq + QKV_SZ + 4096);
    }
    #pragma unroll
    for (int t8 = 0; t8 < 8; ++t8) vf[t8] = LD128(l16 + (t8 & 3) * 1024, oq + 2 * QKV_SZ + (t8 >> 2) * 4096);
    SB();
  };
  u32x4 kmf[4]; u32x2 vmf[8];
  auto issue_km = [&](int s) {
    const unsigned okm = ((l * 64 + s) * 32 + h) * 2048;
    #pragma unroll
    for (int ks = 0; ks < 4; ++ks) kmf[ks] = LD128(kmOff + ks * 512, O_KM + okm);
    #pragma unroll
    for (int t8 = 0; t8 < 8; ++t8) vmf[t8] = LD64(vmOff + t8 * 256, O_VMT + okm);
    SB();
  };
  const unsigned flv = lane < 3 ? (unsigned)lane * 4u : (lane == 3 ? 128u : OOB);
  if (!wait_flags(ws, oFH, 3, 4, lane)) return;
  issue(0);
  bool early = false;
  for (int s = 0; s < 64; ++s) {
    f32x4 s0, s1, s2;
    if (!early) {
      #pragma unroll
      for (int ks = 0; ks < 4; ++ks) {
        s0 = MFMA(BC8(kf[0][ks]), BC8(qf[ks]), ks ? s0 : z);
        s1 = MFMA(BC8(kf[1][ks]), BC8(qf[ks]), ks ? s1 : z);
      }
      SB();
      if (!wait_flags(ws, oFH + s * 8192 + 128, 1, 4, lane)) return;
      issue_km(s);
      TR(3, l, s, tid == 0 && a == 0);
    } else {
      #pragma unroll
      for (int ks = 0; ks < 4; ++ks) {
        s0 = MFMA(BC8(kf[0][ks]), BC8(qf[ks]), ks ? s0 : z);
        s1 = MFMA(BC8(kf[1][ks]), BC8(qf[ks]), ks ? s1 : z);
      }
      SB();
    }
    float sv[12];
    #pragma unroll
    for (int b = 0; b < 4; ++b) {
      sv[b] = (8 * g + b <= tq) ? s0[b] : -INFINITY;
      sv[4 + b] = (8 * g + 4 + b <= tq) ? s1[b] : -INFINITY;
    }
    float mx = fmaxf(fmaxf(fmaxf(sv[0], sv[1]), fmaxf(sv[2], sv[3])), fmaxf(fmaxf(sv[4], sv[5]), fmaxf(sv[6], sv[7])));
    SB();
    #pragma unroll
    for (int ks = 0; ks < 4; ++ks) s2 = MFMA(BC8(kmf[ks]), BC8(qf[ks]), ks ? s2 : z);
    #pragma unroll
    for (int b = 0; b < 4; ++b) { sv[8 + b] = g < 2 ? s2[b] : -INFINITY; mx = fmaxf(mx, sv[8 + b]); }
    mx = red_max(mx);
    const float nm = -mx * cexp;
    float sum = 0;
    #pragma unroll
    for (int b = 0; b < 12; ++b) { sv[b] = __builtin_amdgcn_exp2f(fmaf(sv[b], cexp, nm)); sum += sv[b]; }
    sum = red_sum(sum);
    const float inv = __builtin_amdgcn_rcpf(sum);
    u32x4 pf = {pk(sv[0] * inv, sv[1] * inv), pk(sv[2] * inv, sv[3] * inv), pk(sv[4] * inv, sv[5] * inv), pk(sv[6] * inv, sv[7] * inv)};
    u32x4 pm = {pk(sv[8] * inv, sv[9] * inv), pk(sv[10] * inv, sv[11] * inv), 0u, 0u};
    f32x4 oo[8];
    #pragma unroll
    for (int t8 = 0; t8 < 8; ++t8) oo[t8] = MFMA(BC8(vf[t8]), BC8(pf), z);
    #pragma unroll
    for (int t8 = 0; t8 < 8; ++t8) { u32x4 v0 = {vmf[t8].x, vmf[t8].y, 0u, 0u}; oo[t8] = MFMA(BC8(v0), BC8(pm), oo[t8]); }
    u32x4* ofw = ofx + (s & 1) * 1024;
    #pragma unroll
    for (int ks = 0; ks < 4; ++ks) {
      u32x4 o = {pk(oo[2 * ks][0], oo[2 * ks][1]), pk(oo[2 * ks][2], oo[2 * ks][3]), pk(oo[2 * ks + 1][0], oo[2 * ks + 1][1]), pk(oo[2 * ks + 1][2], oo[2 * ks + 1][3])};
      ofw[(w * 4 + ks) * 64] = o;
    }
    SB();
    if (early) { DRAIN(); signal_cu(ws, cnt, O_FPA + ((l * 64 + s - 1) * 16 + a) * 128, lane); }
    unsigned nfl = LD32(flv, oFH + ((s + 1) & 63) * 8192);
    LBAR();
    f32x4 acc[8][2];
    bf16x8 b0 = BC8(ofw[0]), b1 = BC8(ofw[256]);
    bool pre = false;
    #pragma unroll
    for (int q = 0; q < 8; ++q) {
      const int h2 = q >> 2, ks = q & 3;
      bf16x8 n0, n1;
      if (q < 7) { n0 = BC8(ofw[((((q + 1) >> 2) * 2 + 0) * 4 + ((q + 1) & 3)) * 64]); n1 = BC8(ofw[((((q + 1) >> 2) * 2 + 1) * 4 + ((q + 1) & 3)) * 64]); }
      if (q == 3) { if (s < 63 && __all(lane >= 3 || nfl != EMPTY)) { issue(s + 1); pre = true; } }
      if (q >= 4) {
        #pragma unroll
        for (int c = 4; c < 8; ++c) wo[1][c][ks] = lwo[((c - 4) * 4 + ks) * 64];
      }
      SB();
      #pragma unroll
      for (int c = 0; c < 8; ++c) {
        acc[c][0] = MFMA(wo[h2][c][ks], b0, q ? acc[c][0] : z);
        acc[c][1] = MFMA(wo[h2][c][ks], b1, q ? acc[c][1] : z);
      }
      SB();
      if (q < 7) { b0 = n0; b1 = n1; }
    }
    early = s < 63 && pre && __all(nfl != EMPTY);
    if (early) issue_km(s + 1);
    store_partial(ws, lds + 32768 + w * 8704, acc, O_PA + (s * 16 + a) * 32768, w, lane);
    TR(4, l, s, tid == 0 && a == 0);
    if (!early) {
      DRAIN();
      signal_cu(ws, cnt, O_FPA + ((l * 64 + s) * 16 + a) * 128, lane);
      if (s < 63 && !pre) { if (!wait_flags(ws, oFH + (s + 1) * 8192, 3, 4, lane)) return; issue(s + 1); }
    }
  }
}

__device__ __forceinline__ void role_ffn(const Params& p, rsrc_t ws, char* lds, const int l, const int f) {
  const int tid = threadIdx.x, lane = tid & 63, w = __builtin_amdgcn_readfirstlane(tid >> 6), i = lane & 15, g = lane >> 4;
  u32x4* hx = (u32x4*)(lds + 34816) + lane;
  bf16x8* lwd = (bf16x8*)(lds + 34816 + 8192) + w * 1024 + lane;
  bf16x8 wg[2][2][16], wd[4][4];
  {
    const bf16x8* src = (const bf16x8*)(p.wsp + O_WGU) + (size_t)((f * 4 + w) * 64) * 64 + lane;
    #pragma unroll
    for (int m = 0; m < 2; ++m)
    #pragma unroll
    for (int t = 0; t < 2; ++t) {
      #pragma unroll
      for (int kk = 0; kk < 16; ++kk) wg[m][t][kk] = src[((m * 2 + t) * 16 + kk) * 64];
      SB();
      #pragma unroll
      for (int kk = 0; kk < 16; ++kk) { if ((m * 2 + t) * 16 + kk < 44) PINA(wg[m][t][kk]); }
      SB();
    }
    const bf16x8* sd = (const bf16x8*)(p.wsp + O_WD) + (size_t)(f * 32 + w * 8) * 256 + lane;
    #pragma unroll
    for (int c = 0; c < 4; ++c)
    #pragma unroll
    for (int ks = 0; ks < 4; ++ks) { wd[c][ks] = sd[(c * 4 + ks) * 64]; lwd[(c * 4 + ks) * 64] = sd[((c + 4) * 4 + ks) * 64]; }
  }
  const f32x4 z = {0, 0, 0, 0};
  const char* xb = lds + g * 528 + i * 16;
  const unsigned flv = lane < 32 ? (unsigned)lane * 128u : OOB;
  unsigned* cnt = (unsigned*)(lds + 34816 + 8192 + 65536 + 4 * 8704);
  if (tid == 0) *cnt = 0u;
  u32x4 xr[8];
  auto issue = [&](int s) {
    #pragma unroll
    for (int k = 0; k < 8; ++k) xr[k] = LD128(tid * 16, O_XN1 + (l * 64 + s) * 32768 + k * 4096);
    SB();
  };
  bool early = false;
  for (int s = 0; s < 64; ++s) {
    if (!early) { if (!wait_flags(ws, O_FXN1 + (l * 64 + s) * 4096, 32, 128, lane)) return; issue(s); }
    stage_x(lds, xr, tid);
    LBAR();
    TR(6, l, s, tid == 0 && f == 0);
    f32x4 ag[2][2][2];
    bf16x8 xf[2][2];
    xf[0][0] = XFRAG(xb, 0, 0, 16); xf[0][1] = XFRAG(xb, 0, 1, 16);
    #pragma unroll
    for (int kk = 0; kk < 16; ++kk) {
      if (kk < 15) { xf[(kk + 1) & 1][0] = XFRAG(xb, kk + 1, 0, 16); xf[(kk + 1) & 1][1] = XFRAG(xb, kk + 1, 1, 16); }
      SB();
      #pragma unroll
      for (int m = 0; m < 2; ++m)
      #pragma unroll
      for (int t = 0; t < 2; ++t) {
        ag[m][t][0] = MFMA(wg[m][t][kk], xf[kk & 1][0], kk ? ag[m][t][0] : z);
        ag[m][t][1] = MFMA(wg[m][t][kk], xf[kk & 1][1], kk ? ag[m][t][1] : z);
      }
      SB();
    }
    #pragma unroll
    for (int rt = 0; rt < 2; ++rt) {
      float hv[8];
      #pragma unroll
      for (int t = 0; t < 2; ++t)
      #pragma unroll
      for (int b = 0; b < 4; ++b) {
        const float gv = ag[0][t][rt][b], uv = ag[1][t][rt][b];
        const float y2 = gv * fmaf(gv * gv, -0.044715f * 2.302208198144325f, -2.302208198144325f);
        const float ex = __builtin_amdgcn_exp2f(y2);
        hv[t * 4 + b] = gv * __builtin_amdgcn_rcpf(1.0f + ex) * uv;
      }
      u32x4 o = {pk(hv[0], hv[1]), pk(hv[2], hv[3]), pk(hv[4], hv[5]), pk(hv[6], hv[7])};
      hx[(rt * 4 + w) * 64] = o;
    }
    bf16x8 wl[4];
    #pragma unroll
    for (int c = 0; c < 4; ++c) wl[c] = lwd[(c * 4) * 64];
    if (early) { DRAIN(); signal_cu(ws, cnt, O_FPF + ((l * 64 + s - 1) * 16 + f) * 128, lane); }
    unsigned nfl = LD32(flv, O_FXN1 + (l * 64 + ((s + 1) & 63)) * 4096);
    LBAR();
    f32x4 acc[8][2];
    bf16x8 b0 = BC8(hx[0]), b1 = BC8(hx[256]);
    #pragma unroll
    for (int ks = 0; ks < 4; ++ks) {
      bf16x8 n0, n1, wn[4];
      if (ks < 3) { n0 = BC8(hx[(ks + 1) * 64]); n1 = BC8(hx[(5 + ks) * 64]);
        #pragma unroll
        for (int c = 0; c < 4; ++c) wn[c] = lwd[(c * 4 + ks + 1) * 64]; }
      SB();
      #pragma unroll
      for (int c = 0; c < 8; ++c) {
        acc[c][0] = MFMA(c < 4 ? wd[c & 3][ks] : wl[c & 3], b0, ks ? acc[c][0] : z);
        acc[c][1] = MFMA(c < 4 ? wd[c & 3][ks] : wl[c & 3], b1, ks ? acc[c][1] : z);
      }
      SB();
      if (ks < 3) { b0 = n0; b1 = n1;
        #pragma unroll
        for (int c = 0; c < 4; ++c) wl[c] = wn[c]; }
    }
    early = s < 63 && __all(nfl != EMPTY);
    if (early) issue(s + 1);
    store_partial(ws, lds + 34816 + 8192 + 65536 + w * 8704, acc, O_PF + (s * 14 + f) * 32768, w, lane);
    TR(7, l, s, tid == 0 && f == 0);
    if (!early) { DRAIN(); signal_cu(ws, cnt, O_FPF + ((l * 64 + s) * 16 + f) * 128, lane); }
  }
}

__device__ __forceinline__ u32x4 ln_row(const float (&v)[8]) {
  float s = ((v[0] + v[1]) + (v[2] + v[3])) + ((v[4] + v[5]) + (v[6] + v[7]));
  const float mean = wave_sum(s) * (1.0f / 512.0f);
  float q = 0;
  #pragma unroll
  for (int j = 0; j < 8; ++j) { float d = v[j] - mean; q += d * d; }
  const float var = wave_sum(q) * (1.0f / 512.0f);
  const float mb = rb(mean), den = rb(sqrtf(rb(rb(var) + 1e-5f)));
  float y[8];
  #pragma unroll
  for (int j = 0; j < 8; ++j) y[j] = rb(v[j] - mb) / den;
  u32x4 o = {pk(y[0], y[1]), pk(y[2], y[3]), pk(y[4], y[5]), pk(y[6], y[7])};
  return o;
}
__device__ __forceinline__ void role_red(const Params& p, rsrc_t ws, char* lds, const int r) {
  const int tid = threadIdx.x, lane = tid & 63, w = __builtin_amdgcn_readfirstlane(tid >> 6);
  u32x4* xres = (u32x4*)lds + lane;
  if (p.role_mask & 32) {
    u32x4 xr[16];
    #pragma unroll
    for (int k = 0; k < 16; ++k) xr[k] = *(const u32x4*)(p.x + ((size_t)((k * 4 + w) * 32 + r) * 512 + lane * 8));
    #pragma unroll
    for (int k = 0; k < 16; ++k) {
      const int s = k * 4 + w;
      float v[8] = {lo16(xr[k].x), hi16(xr[k].x), lo16(xr[k].y), hi16(xr[k].y), lo16(xr[k].z), hi16(xr[k].z), lo16(xr[k].w), hi16(xr[k].w)};
      ST128(ln_row(v), lane * 16, O_XN + s * 32768 + r * 1024);
      DRAIN();
      if (lane == 0) ST32(1u, r * 128, O_FXN + s * 4096);
    }
  }
  const int st = w & 1, l = w >> 1;
  if (!(p.layer_mask & (1 << l)) || !(p.role_mask & (st ? 16 : 8))) return;
  const int np = st ? 14 : 16;
  for (int s = 0; s < 64; ++s) {
    u32x4 xv;
    if (w == 0) xv = *(const u32x4*)(p.x + ((size_t)(s * 32 + r) * 512 + lane * 8));
    if (!wait_flags(ws, (st ? O_FPF : O_FPA) + (l * 64 + s) * 2048, np, 128, lane)) return;
    TR(st ? 10 : 9, l, s, lane == 0 && r == 0);
    const unsigned ob = (st ? O_PF + s * 14 * 32768 : O_PA + s * 16 * 32768) + r * 1024;
    u32x4 a[16];
    #pragma unroll
    for (int k = 0; k < 16; ++k) { if (k < 14 || !st) a[k] = LD128(lane * 16, ob + k * 32768); else a[k] = (u32x4){0u, 0u, 0u, 0u}; }
    if (w > 0) xv = xres[s * 64];
    f32x4 sa = {0, 0, 0, 0}, sb = {0, 0, 0, 0};
    #pragma unroll
    for (int k = 0; k < 16; ++k) {
      sa += (f32x4){lo16(a[k].x), hi16(a[k].x), lo16(a[k].y), hi16(a[k].y)};
      sb += (f32x4){lo16(a[k].z), hi16(a[k].z), lo16(a[k].w), hi16(a[k].w)};
    }
    float v[8];
    v[0] = rb(lo16(xv.x) + rb(sa[0])); v[1] = rb(hi16(xv.x) + rb(sa[1])); v[2] = rb(lo16(xv.y) + rb(sa[2])); v[3] = rb(hi16(xv.y) + rb(sa[3]));
    v[4] = rb(lo16(xv.z) + rb(sb[0])); v[5] = rb(hi16(xv.z) + rb(sb[1])); v[6] = rb(lo16(xv.w) + rb(sb[2])); v[7] = rb(hi16(xv.w) + rb(sb[3]));
    u32x4 xo = {pk(v[0], v[1]), pk(v[2], v[3]), pk(v[4], v[5]), pk(v[6], v[7])};
    if (w == 3) {
      *(u32x4*)(p.out + ((size_t)(s * 32 + r) * 512 + lane * 8)) = xo;
    } else {
      xres[s * 64] = xo;
      const unsigned od = st ? O_XN + (64 + s) * 32768 : O_XN1 + (l * 64 + s) * 32768;
      ST128(ln_row(v), lane * 16, od + r * 1024);
      asm volatile("s_waitcnt vmcnt(0) lgkmcnt(0)" ::: "memory");
      if (lane == 0) ST32(1u, r * 128, st ? O_FXN + (64 + s) * 4096 : O_FXN1 + (l * 64 + s) * 4096);
    }
    TR(st ? 8 : 5, l, s, lane == 0 && r == 0);
  }
}

__global__ void __launch_bounds__(256, 1) k_main(Params p) {
  extern __shared__ __attribute__((aligned(16))) char lds[];
  rsrc_t ws = __builtin_amdgcn_make_buffer_rsrc(p.wsp, 0, 256u * 1024 * 1024, 0x00020000);
  const int b = blockIdx.x;
  TR(15, 0, 0, b == 0 && threadIdx.x == 0);
  if (b < 64) { if ((p.role_mask & 1) && (p.layer_mask & (1 << (b >> 5)))) role_chain(p, ws, lds, b >> 5, b & 31); }
  else if (b < 96) { if (p.role_mask & 2) role_qkv<2>(p, ws, lds, 2, b & 31); }
  else if (b < 128) { if (p.role_mask & 2) role_qkv<1>(p, ws, lds, 1, b & 31); }
  else if (b < 160) { if (p.role_mask & 2) role_qkv<0>(p, ws, lds, 0, b & 31); }
  else if (b < 192) { if ((p.role_mask & 4) && (p.layer_mask & (1 << ((b - 160) >> 4)))) role_att(p, ws, lds, (b - 160) >> 4, b & 15); }
  else if (b < 220) { const int l = (b - 192) >= 14; if ((p.role_mask & 64) && (p.layer_mask & (1 << l))) role_ffn(p, ws, lds, l, b - 192 - 14 * l); }
  else if (b < 252) { if (p.role_mask & (8 | 16 | 32)) role_red(p, ws, lds, b - 220); }
}

extern "C" void kernel_launch(void* const* d_in, const int* in_sizes, int n_in, void* d_out, int out_size, void* d_ws, size_t ws_size, hipStream_t stream) {
  const u16* x = (const u16*)d_in[0]; const u16* mem0 = (const u16*)d_in[1];
  const u16* Wq = (const u16*)d_in[2]; const u16* Wk = (const u16*)d_in[3]; const u16* Wv = (const u16*)d_in[4]; const u16* Wo = (const u16*)d_in[5];
  const u16* wg = (const u16*)d_in[8]; const u16* wu = (const u16*)d_in[9]; const u16* wd = (const u16*)d_in[10];
  (void)hipFuncSetAttribute((const void*)k_main, hipFuncAttributeMaxDynamicSharedMemorySize, LDS_BYTES);
  k_prep<<<PREP_B + INIT_B, 256, 0, stream>>>(Wq, Wk, Wv, Wo, wg, wu, wd, (char*)d_ws);
  Params p{x, mem0, (u16*)d_out, (char*)d_ws, 127 | g_delay, 3};
  k_main<<<256, 256, LDS_BYTES, stream>>>(p);
}
```

```cpp
#include <hip/hip_runtime.h>
#include <hip/hip_bf16.h>
#include <stdint.h>

typedef unsigned short u16;
typedef short bf16x8 __attribute__((ext_vector_type(8)));
typedef float f32x4 __attribute__((ext_vector_type(4)));
typedef float f32x2 __attribute__((ext_vector_type(2)));
typedef unsigned u32x4 __attribute__((ext_vector_type(4)));
typedef unsigned u32x2 __attribute__((ext_vector_type(2)));
typedef __bf16 bfx2 __attribute__((ext_vector_type(2)));

constexpr unsigned O_FXN  = 0;
constexpr unsigned O_FXN1 = O_FXN + 524288;
constexpr unsigned O_FPA  = O_FXN1 + 524288;
constexpr unsigned O_FPF  = O_FPA + 262144;
constexpr unsigned O_ABT  = O_FPF + 262144;
constexpr unsigned O_FH   = O_ABT + 256;
constexpr unsigned O_MEM  = O_FH + 1048576;
constexpr unsigned O_PC   = O_MEM + 1048576;
#define PCRING 8
constexpr unsigned CTRL_BYTES = (O_PC + PCRING * 1048576 + 16383) & ~16383u;
constexpr unsigned O_WQKV = (CTRL_BYTES + 4095) & ~4095u;
constexpr unsigned O_WO   = O_WQKV + 3 * 512 * 4096 * 2;
constexpr unsigned O_WGU  = O_WO + 4096 * 512 * 2;
constexpr unsigned O_WD   = O_WGU + 2 * 512 * 1792 * 2;
constexpr unsigned O_XN   = O_WD + 1792 * 512 * 2;
constexpr unsigned O_XN1  = O_XN + 2 * 64 * 32768;
constexpr unsigned O_Q    = O_XN1 + 2 * 64 * 32768;
constexpr unsigned QKV_SZ = 64 * 32 * 8192;
constexpr unsigned O_KM   = O_Q + 3 * QKV_SZ;
constexpr unsigned O_VMT  = O_KM + 2 * 64 * 32 * 2048;
constexpr unsigned O_PA   = O_VMT + 2 * 64 * 32 * 2048;
constexpr unsigned O_PF   = O_PA + 64u * 16 * 32768;
constexpr unsigned WS_END = O_PF + 64u * 14 * 32768;
static_assert(WS_END <= 256u * 1024 * 1024, "workspace");

constexpr int LDS_BYTES = 160 * 1024;
#define SC1 16
#define OOB 0x7fffff00u
#define EMPTY 0xffffffffu
#define SPIN_CAP (1u << 20)
#define DELAY_A 10
#define DELAY_B 148

#define QKV_AHEAD 10
#define QKV_URGENT 4
#define POLL_SLEEP 4
#define KB_VAL DELAY_B
#define SLEEPN(c, r) __builtin_amdgcn_s_sleep(c)
#define g_delay 0
#define PSLOT(l, s) (s)
#define SB() __builtin_amdgcn_sched_barrier(0)
#define MFMA(a, b, c) __builtin_amdgcn_mfma_f32_16x16x32_bf16(a, b, c, 0, 0, 0)
#define BC8(x) __builtin_bit_cast(bf16x8, x)
#define MA0(acc, A, B) asm volatile("v_mfma_f32_16x16x32_bf16 %0, %1, %2, 0" : "=v"(acc) : "a"(A), "v"(B))
#define MA1(acc, A, B) asm volatile("v_mfma_f32_16x16x32_bf16 %0, %1, %2, %0" : "+v"(acc) : "a"(A), "v"(B))
#define MV0(acc, A, B) asm volatile("v_mfma_f32_16x16x32_bf16 %0, %1, %2, 0" : "=v"(acc) : "v"(A), "v"(B))
#define MV1(acc, A, B) asm volatile("v_mfma_f32_16x16x32_bf16 %0, %1, %2, %0" : "+v"(acc) : "v"(A), "v"(B))
#define NOP(n) asm volatile("s_nop %0" :: "i"(n))
#define PINA(x) asm volatile("" : "+a"(x))
#define PINV(x) asm volatile("" : "+v"(x))
#define DPP8(x) ((unsigned)__builtin_amdgcn_update_dpp(0, (int)(x), 0x128, 0xf, 0xf, false))
#define DRAIN() asm volatile("s_waitcnt vmcnt(0)" ::: "memory")
#define MEMFENCE() asm volatile("" ::: "memory")
#define LBAR() do { asm volatile("s_waitcnt lgkmcnt(0)" ::: "memory"); __builtin_amdgcn_s_barrier(); asm volatile("" ::: "memory"); } while (0)
#define LD128(off, so) __builtin_amdgcn_raw_buffer_load_b128(ws, (off), (so), SC1)
#define LD64(off, so) __builtin_amdgcn_raw_buffer_load_b64(ws, (off), (so), SC1)
#define LD32(off, so) __builtin_amdgcn_raw_buffer_load_b32(ws, (off), (so), SC1)
#define ST128(v, off, so) __builtin_amdgcn_raw_buffer_store_b128((v), ws, (off), (so), SC1)
#define ST64(v, off, so) __builtin_amdgcn_raw_buffer_store_b64((v), ws, (off), (so), SC1)
#define ST32(v, off, so) __builtin_amdgcn_raw_buffer_store_b32((v), ws, (off), (so), SC1)
typedef __amdgpu_buffer_rsrc_t rsrc_t;

__device__ __forceinline__ unsigned pk(float a, float b) { f32x2 v = {a, b}; bfx2 r = __builtin_convertvector(v, bfx2); return __builtin_bit_cast(unsigned, r); }
__device__ __forceinline__ float rb(float a) { return __uint_as_float(pk(a, a) << 16); }
__device__ __forceinline__ float lo16(unsigned u) { return __uint_as_float(u << 16); }
__device__ __forceinline__ float hi16(unsigned u) { return __uint_as_float(u & 0xffff0000u); }
__device__ __forceinline__ float red_max(float x) {
  auto r = __builtin_amdgcn_permlane32_swap(__float_as_uint(x), __float_as_uint(x), false, false);
  x = fmaxf(__uint_as_float(r[0]), __uint_as_float(r[1]));
  auto q = __builtin_amdgcn_permlane16_swap(__float_as_uint(x), __float_as_uint(x), false, false);
  return fmaxf(__uint_as_float(q[0]), __uint_as_float(q[1]));
}
__device__ __forceinline__ float red_sum(float x) {
  auto r = __builtin_amdgcn_permlane32_swap(__float_as_uint(x), __float_as_uint(x), false, false);
  x = __uint_as_float(r[0]) + __uint_as_float(r[1]);
  auto q = __builtin_amdgcn_permlane16_swap(__float_as_uint(x), __float_as_uint(x), false, false);
  return __uint_as_float(q[0]) + __uint_as_float(q[1]);
}
__device__ __forceinline__ float wave_sum(float x) {
  x += __uint_as_float((unsigned)__builtin_amdgcn_update_dpp(0, (int)__float_as_uint(x), 0x121, 0xf, 0xf, false));
  x += __uint_as_float((unsigned)__builtin_amdgcn_update_dpp(0, (int)__float_as_uint(x), 0x122, 0xf, 0xf, false));
  x += __uint_as_float((unsigned)__builtin_amdgcn_update_dpp(0, (int)__float_as_uint(x), 0x124, 0xf, 0xf, false));
  x += __uint_as_float(DPP8(__float_as_uint(x)));
  return red_sum(x);
}

__device__ __forceinline__ bool wait_flags(rsrc_t ws, unsigned off, int n, unsigned stride, int lane) {
  unsigned vo = lane < n ? (unsigned)lane * stride : OOB;
  for (unsigned spins = 0; spins < SPIN_CAP; ++spins) {
    MEMFENCE();
    unsigned v = LD32(vo, off);
    unsigned long long miss = __ballot(v == EMPTY);
    if (!miss) return true;
    const int fm = __builtin_ctzll(miss);
    const unsigned v1 = lane == fm ? vo : OOB;
    if (v != EMPTY) vo = OOB;
    for (; spins < SPIN_CAP; ++spins) {
      __builtin_amdgcn_s_sleep(POLL_SLEEP);
      MEMFENCE();
      if (__all(LD32(v1, off) != EMPTY)) break;
      if ((spins & 63u) == 63u) { if (__builtin_amdgcn_readfirstlane(LD32(0u, O_ABT)) == 1u) return false; }
    }
    if (lane == fm) vo = OOB;
  }
  if (lane == 0) ST32(1u, 0u, O_ABT);
  return false;
}

__device__ __forceinline__ void signal_cu(rsrc_t ws, unsigned* cnt, unsigned off, int lane) {
  if (lane == 0) { unsigned old = atomicAdd(cnt, 1u); if ((old & 3u) == 3u) ST32(1u, 0u, off); }
}
#define ST(k)
#define SK(k)
#define TR(ev, l, s, cond)
#define SXDECL unsigned long long acc_x[12] = {0,0,0,0,0,0,0,0,0,0,0,0}; unsigned long long tprevx = __builtin_amdgcn_s_memtime();
#define SXX(k) do { SB(); unsigned long long t_; asm volatile("s_memtime %0\n\ts_waitcnt lgkmcnt(0)" : "=s"(t_) :: "memory"); acc_x[k] += t_ - tprevx; tprevx = t_; SB(); } while (0)
#define SXOUT(cond) do { if (cond) for (int k = 0; k < 12; ++k) { u32x2 v_ = {(unsigned)acc_x[k], (unsigned)(acc_x[k] >> 32)}; __builtin_amdgcn_raw_buffer_store_b64(v_, ws, 0u, WS_END + 5242880 + (l * 16 + k) * 8, 0); } } while (0)
#define SA(k)
#define SF(k)
struct Params {
  const u16* x; const u16* mem0; u16* out; char* wsp; int role_mask; int layer_mask;
};

constexpr int PB1 = 3 * 16 * 8, PB2 = 128, PB3 = 2 * 16 * 4, PB4 = 56;
constexpr int PREP_B = PB1 + PB2 + PB3 + PB4, INIT_B = CTRL_BYTES / 16384;
__global__ void __launch_bounds__(256) k_prep(const u16* Wq, const u16* Wk, const u16* Wv, const u16* Wo, const u16* wg, const u16* wu, const u16* wd, char* wsp) {
  __shared__ u32x4 t[2048];
  int b = blockIdx.x; const int tid = threadIdx.x;
  if (b >= PREP_B) {
    const u32x4 e = {EMPTY, EMPTY, EMPTY, EMPTY};
    #pragma unroll
    for (int k = 0; k < 4; ++k) ((u32x4*)wsp)[(b - PREP_B) * 1024 + k * 256 + tid] = e;
    return;
  }
  const u16* src; int ld, kk, np, nwin = 16; char* dst; int wstride, tstride;
  if (b < PB1) {
    const int m = b >> 7, id = b & 127; kk = id >> 3; np = id & 7;
    src = m == 0 ? Wq : (m == 1 ? Wk : Wv); ld = 4096;
    dst = wsp + O_WQKV + (size_t)((np * 16 * 3 + m) * 32 + kk) * 1024; wstride = 3 * 32768; tstride = 16384;
  } else if (b < PB1 + PB2) {
    kk = b - PB1; np = 0; src = Wo; ld = 512;
    dst = wsp + O_WO + (size_t)((kk >> 2) * 128 + (kk & 3)) * 1024; wstride = 8192; tstride = 4096;
  } else if (b < PB1 + PB2 + PB3) {
    const int id = b - PB1 - PB2; const int m = id >> 6; kk = (id >> 2) & 15; np = id & 3;
    src = m == 0 ? wg : wu; ld = 1792; if (np == 3) nwin = 8;
    dst = wsp + O_WGU + (size_t)((np * 16 * 2 + m) * 32 + kk) * 1024; wstride = 2 * 32768; tstride = 16384;
  } else {
    kk = b - PB1 - PB2 - PB3; np = 0; src = wd; ld = 512;
    dst = wsp + O_WD + (size_t)((kk >> 2) * 128 + (kk & 3)) * 1024; wstride = 8192; tstride = 4096;
  }
  const int g = tid >> 6, n8 = tid & 63;
  if ((n8 >> 2) < nwin) {
    u32x4 r[8];
    #pragma unroll
    for (int j = 0; j < 8; ++j) r[j] = *(const u32x4*)(src + (size_t)(kk * 32 + g * 8 + j) * ld + np * 512 + n8 * 8);
    #pragma unroll
    for (int c = 0; c < 8; ++c) {
      u32x4 o;
      #pragma unroll
      for (int q = 0; q < 4; ++q) {
        const unsigned lo = r[2 * q][c >> 1], hi = r[2 * q + 1][c >> 1];
        o[q] = (c & 1) ? ((lo >> 16) | (hi & 0xffff0000u)) : ((lo & 0xffffu) | (hi << 16));
      }
      t[((n8 >> 2) * 2 + (c >> 2)) * 64 + g * 16 + (n8 & 3) * 4 + (c & 3)] = o;
    }
  }
  __syncthreads();
  #pragma unroll
  for (int k = 0; k < 8; ++k) {
    const int f = k * 4 + g;
    if ((f >> 1) < nwin) *(u32x4*)(dst + (size_t)(f >> 1) * wstride + (f & 1) * tstride + n8 * 16) = t[f * 64 + n8];
  }
}

__device__ __forceinline__ void role_chain(const Params& p, rsrc_t ws, char* lds, const int l, const int h) {
  const int tid = threadIdx.x, lane = tid & 63, w = __builtin_amdgcn_readfirstlane(tid >> 6), i = lane & 15, g = lane >> 4;
  const int vi = g * 8 + (i & 7);
  bf16x8* ldsWo = (bf16x8*)lds;
  bf16x8* memf = (bf16x8*)(lds + 131072);
  f32x4* sc = (f32x4*)(lds + 131072 + 8192);
  bf16x8* of = (bf16x8*)(lds + 131072 + 8192 + 7168);
  bf16x8* wx = (bf16x8*)(lds + 131072 + 8192 + 7168 + 2048) + w * 192 + lane;
  unsigned* cnt = (unsigned*)(lds + 131072 + 8192 + 7168 + 2048 + 12288);
  bf16x8 wf[3][2][16];
  {
    const bf16x8* src = (const bf16x8*)(p.wsp + O_WQKV) + (size_t)((h * 4 + w) * 96) * 64 + lane;
    #pragma unroll
    for (int m = 0; m < 3; ++m)
    #pragma unroll
    for (int t = 0; t < 2; ++t) {
      #pragma unroll
      for (int kk = 0; kk < 16; ++kk) { wf[m][t][kk] = src[((m * 2 + t) * 16 + kk) * 64]; }
      if (m == 2) { wx[(t * 2 + 0) * 64] = wf[m][t][14]; if (t == 0) wx[64] = wf[m][t][15]; }
      SB();
      #pragma unroll
      for (int kk = 0; kk < 16; ++kk) { if (m < 2) PINA(wf[m][t][kk]); }
      SB();
    }
    const bf16x8* so = (const bf16x8*)(p.wsp + O_WO) + (size_t)h * 8192;
    for (int k = 0; k < 32; ++k) ldsWo[k * 256 + tid] = so[k * 256 + tid];
  }
  rsrc_t rM0 = __builtin_amdgcn_make_buffer_rsrc((void*)p.mem0, 0, 8192, 0x00020000);
  const int prm = 8 * (i >> 2) + (i & 3);
  const int kOff = w * 1024 + lane * 16;
  const int vOff = w * 2048 + lane * 16;
  const unsigned oK = O_Q + QKV_SZ + h * 8192, oVT = O_Q + 2 * QKV_SZ + h * 8192;
  const unsigned oFH = O_FH + (l * 64 * 32 + h) * 256;
  const unsigned oKM = O_KM + (l * 64 * 32 + h) * 2048, oVMT = O_VMT + (l * 64 * 32 + h) * 2048;
  const unsigned oMEM = O_MEM + l * 64 * 8192, oPC0 = O_PC + l * 32 * 32 * 512;
  u32x4 kx[2], vx[2];
  if (tid == 0) *cnt = 0u;
  if (!wait_flags(ws, oFH + 4, 2, 4, lane)) return;
  #pragma unroll
  for (int t = 0; t < 2; ++t) { kx[t] = LD128(kOff, oK + t * 4096); vx[t] = LD128(vOff + t * 1024, oVT); }
  __syncthreads();
  const int hi = i >> 3;
  const bf16x8* mfp = memf + g * 8 + (i & 7);
  const bf16x8* wop = ldsWo + w * 2048 + lane;
  const f32x4* scx = sc + hi * 32 + vi;
  const f32x2* scm = (const f32x2*)(sc + 64 + vi) + hi;
  f32x4* scw = sc + w * 96 + vi;
  u32x4* ofw = hi ? (u32x4*)(sc) + 384 + lane : (u32x4*)of + w * 32 + vi;
  const bf16x8* ofr_p = mfp + 960;
  const int tid16 = tid * 16;
  const unsigned pcwOff = vi * 16 + hi * 16384;
  const unsigned kmOff = hi ? OOB : (unsigned)((w * 32 + vi) * 16);
  const unsigned vmOff = g < 2 ? (unsigned)(w * 512 + lane * 8) : OOB;
  const int pcrOff = (lane >> 3) * 2048 + (w * 8 + (lane & 7)) * 16;
  const unsigned memOff = lane < 8 ? (unsigned)(w * 128 + lane * 16) : OOB;
  const unsigned flOff = lane < 2 ? (unsigned)(lane * 4 + 4) : OOB;
  const float cexp = 0.08838834764831845f * 1.4426950408889634f;
  int nsleep = 0;
  for (int s = 0; s < 64; ++s) {
    {
      u32x4 x[2];
      if (s == 0) {
        #pragma unroll
        for (int k = 0; k < 2; ++k) { int c = k * 256 + tid; x[k] = __builtin_amdgcn_raw_buffer_load_b128(rM0, ((c & 7) * 512 + (c >> 3) * 8) * 2, 0, 0); }
      } else {
        for (int d_ = 0; d_ < nsleep; ++d_) __builtin_amdgcn_s_sleep(1);
        unsigned spins = 0;
        for (; spins < SPIN_CAP; ++spins) {
          MEMFENCE();
          #pragma unroll
          for (int k = 0; k < 2; ++k) x[k] = LD128(tid16, oMEM + s * 8192 + k * 4096);
          bool ok = true;
          #pragma unroll
          for (int k = 0; k < 2; ++k) ok &= (x[k].x != EMPTY) && (x[k].y != EMPTY) && (x[k].z != EMPTY) && (x[k].w != EMPTY);
          if (__all(ok)) break;
        }
        if (spins >= SPIN_CAP && lane == 0) ST32(1u, 0u, O_ABT);
      }
      ST(0); SK(0);
      #pragma unroll
      for (int k = 0; k < 2; ++k) ((u32x4*)memf)[tid + k * 256] = x[k];
    }
    const unsigned tstamp = ((unsigned)__builtin_amdgcn_s_memrealtime() >> 1) & 255u;
    LBAR();
    TR(0, l, s, tid == 0 && h == 0);
    ST(1);
    f32x4 aq[2], ak[2], av[2];
    bf16x8 mf[16];
    #pragma unroll
    for (int kk = 0; kk < 16; ++kk) mf[kk] = mfp[kk * 32];
    SB();
#define QK1(acc, m, kk, t) do { if ((kk) == 0) MA0(acc[t], wf[m][t][kk], mf[kk]); else MA1(acc[t], wf[m][t][kk], mf[kk]); SB(); } while (0)
#define V1(kk, t) do { if ((kk) == 0) MV0(av[t], mf[kk], wf[2][t][kk]); else MV1(av[t], mf[kk], wf[2][t][kk]); SB(); } while (0)
#define RL(kk)
#define K2(kk) do { QK1(ak, 1, kk, 0); QK1(ak, 1, kk, 1); RL(kk); } while (0)
    #pragma unroll
    for (int kk = 0; kk < 16; ++kk) { QK1(aq, 0, kk, 0); QK1(aq, 0, kk, 1); RL(kk); }
    ST(2);
#define KA(kk) QK1(ak, 1, kk, 0)
#define KB(kk) QK1(ak, 1, kk, 1)
    KA(0); KB(0); KA(1); KB(1); KA(2); KB(2); KA(3); KB(3);
    u32x4 qf = {pk(aq[0][0], aq[0][1]), pk(aq[0][2], aq[0][3]), pk(aq[1][0], aq[1][1]), pk(aq[1][2], aq[1][3])}; SB();
    KA(4); KB(4);
    f32x4 s0, s1, s2;
    MV0(s0, kx[0], qf); SB();
    MV0(s1, kx[1], qf); SB();
    KA(5); KB(5); KA(6); KB(6); KA(7); KB(7); KA(8); KB(8);
    scw[0] = s0; SB(); scw[32] = s1; SB();
    KA(9); KB(9); KA(10);
    LBAR();
    f32x4 xa = scx[0], xb = scx[96], xc = scx[192], xd = scx[288];
    SB();
    KB(10); KA(11); KB(11); KA(12); KB(12);
    unsigned nfl = LD32(flOff, oFH + ((s + 1) & 63) * 8192); SB();
    KA(13);
    xa = xa + xb; SB();
    KB(13);
    xc = xc + xd; SB();
    KA(14);
    xa = xa + xc; SB();
    KB(14);
    float mx, tm;
    asm volatile("v_max3_f32 %0, %1, %2, %3\n\tv_max_f32 %0, %0, %4" : "=&v"(mx) : "v"(xa[0]), "v"(xa[1]), "v"(xa[2]), "v"(xa[3])); SB();
    KA(15); KB(15);
    ST(3);
    asm volatile("v_max_f32_dpp %0, %0, %0 row_ror:8 row_mask:0xf bank_mask:0xf\n\tv_mov_b32 %1, %0" : "+v"(mx), "=&v"(tm)); SB();
    V1(0, 0); V1(0, 1);
    asm volatile("v_permlane32_swap_b32 %0, %1\n\tv_max_f32 %0, %0, %1\n\tv_mov_b32 %1, %0" : "+v"(mx), "+v"(tm)); SB();
    V1(1, 0); V1(1, 1);
    asm volatile("v_permlane16_swap_b32 %0, %1\n\tv_max_f32 %0, %0, %1" : "+v"(mx), "+v"(tm)); SB();
    float nm = -mx * cexp; PINV(nm); SB();
    V1(2, 0);
    u32x4 kf = {pk(ak[0][0], ak[0][1]), pk(ak[0][2], ak[0][3]), pk(ak[1][0], ak[1][1]), pk(ak[1][2], ak[1][3])}; SB();
    V1(2, 1); V1(3, 0);
    MV0(s2, kf, qf); SB();
    float e0 = __builtin_amdgcn_exp2f(fmaf(xa[0], cexp, nm)), e1 = __builtin_amdgcn_exp2f(fmaf(xa[1], cexp, nm)); PINV(e0); PINV(e1); SB();
    V1(3, 1);
    float e2 = __builtin_amdgcn_exp2f(fmaf(xa[2], cexp, nm)), e3 = __builtin_amdgcn_exp2f(fmaf(xa[3], cexp, nm)); PINV(e2); PINV(e3); SB();
    V1(4, 0);
    scw[64] = s2; SB();
    unsigned p0 = pk(e0, e1), p1 = pk(e2, e3); PINV(p0); PINV(p1); SB();
    V1(4, 1);
    u32x4 pf = {p0, p1, DPP8(p0), DPP8(p1)}; PINV(pf); SB();
    V1(5, 0);
    u32x4 ones;
    asm volatile("v_mov_b32 %0, 0x3f803f80\n\tv_mov_b32 %1, 0x3f803f80\n\tv_mov_b32 %2, 0x3f803f80\n\tv_mov_b32 %3, 0x3f803f80" : "=v"(ones.x), "=v"(ones.y), "=v"(ones.z), "=v"(ones.w)); PINV(ones); SB();
    V1(5, 1);
    LBAR();
    f32x2 ma = scm[0], mb = scm[192], mc = scm[384], md = scm[576];
    wf[2][0][14] = wx[0]; wf[2][1][14] = wx[128]; wf[2][0][15] = wx[64];
    SB();
    f32x4 ao[2], al;
    MV0(ao[0], vx[0], pf); SB();
    MV0(ao[1], vx[1], pf); SB();
    MV0(al, ones, pf); SB();
    V1(6, 0);
    ST128(kf, kmOff, oKM + s * 65536); SB();
    V1(6, 1); V1(7, 0);
    ma = ma + mb; mc = mc + md; SB();
    V1(7, 1);
    ma = ma + mc;
    if (g >= 2) { ma[0] = -INFINITY; ma[1] = -INFINITY; }
    SB();
    V1(8, 0);
    float mm;
    asm volatile("v_max3_f32 %0, %1, %2, %3" : "=&v"(mm) : "v"(ma[0]), "v"(ma[1]), "v"(mx)); SB();
    V1(8, 1); V1(9, 0);
    asm volatile("v_max_f32_dpp %0, %0, %0 row_ror:8 row_mask:0xf bank_mask:0xf\n\tv_mov_b32 %1, %0" : "+v"(mm), "=&v"(tm)); SB();
    V1(9, 1); V1(10, 0);
    asm volatile("v_permlane32_swap_b32 %0, %1\n\tv_max_f32 %0, %0, %1\n\tv_mov_b32 %1, %0" : "+v"(mm), "+v"(tm)); SB();
    V1(10, 1); V1(11, 0);
    asm volatile("v_permlane16_swap_b32 %0, %1\n\tv_max_f32 %0, %0, %1" : "+v"(mm), "+v"(tm)); SB();
    float nm2 = -mm * cexp; PINV(nm2); SB();
    V1(11, 1);
    float e4 = __builtin_amdgcn_exp2f(fmaf(ma[0], cexp, nm2)), e5 = __builtin_amdgcn_exp2f(fmaf(ma[1], cexp, nm2)); PINV(e4); PINV(e5); SB();
    V1(12, 0);
    float fx = __builtin_amdgcn_exp2f(fmaf(mx, cexp, nm2));
    unsigned p2 = pk(e4, e5); PINV(fx); PINV(p2); SB();
    V1(12, 1);
    u32x4 pm4 = {p2, DPP8(p2), 0u, 0u}; PINV(pm4); SB();
    V1(13, 0);
    al[0] *= fx; PINV(al); SB();
    V1(13, 1);
    ao[0] *= fx; PINV(ao[0]); SB();
    V1(14, 0);
    MV1(al, ones, pm4); SB();
    ao[1] *= fx; PINV(ao[1]); SB();
    V1(14, 1); V1(15, 0); V1(15, 1);
    ST(4);
    bf16x8 wo[16];
    #pragma unroll
    for (int k = 0; k < 7; ++k) wo[k] = wop[k * 64];
    SB();
    u32x4 vm0 = {pk(av[0][0], av[0][1]), pk(av[0][2], av[0][3]), 0u, 0u}; SB();
    u32x4 vm1 = {pk(av[1][0], av[1][1]), pk(av[1][2], av[1][3]), 0u, 0u}; SB();
    MV1(ao[0], vm0, pm4); SB();
    float inv = __builtin_amdgcn_rcpf(al[0]); PINV(inv); SB();
    MV1(ao[1], vm1, pm4); SB();
    #pragma unroll
    for (int k = 7; k < 11; ++k) wo[k] = wop[k * 64];
    NOP(3);
    SB();
    u32x4 ofr = {pk(ao[0][0] * inv, ao[0][1] * inv), pk(ao[0][2] * inv, ao[0][3] * inv), pk(ao[1][0] * inv, ao[1][1] * inv), pk(ao[1][2] * inv, ao[1][3] * inv)};
    *ofw = ofr;
    SB();
    #pragma unroll
    for (int k = 11; k < 16; ++k) wo[k] = wop[k * 64];
    { u32x2 t0 = {vm0.x, vm0.y}, t1 = {vm1.x, vm1.y};
      ST64(t0, vmOff, oVMT + s * 65536);
      ST64(t1, vmOff + 256, oVMT + s * 65536); }
    if (s == 63) break;
    SB();
    LBAR();
    ST(5);
    bf16x8 ob[4];
    #pragma unroll
    for (int ks = 0; ks < 4; ++ks) ob[ks] = ofr_p[ks * 32];
    SB();
    const unsigned tag = (s / PCRING) & 1, tag0 = tag | (tstamp << 1);
    const unsigned oPC = oPC0 + (s % PCRING) * 1048576;
    f32x4 accA[2], accB[2];
    #pragma unroll
    for (int pr = 0; pr < 5; ++pr) {
      u32x4 o;
      #pragma unroll
      for (int ks = 0; ks < 4; ++ks) {
        if (pr < 4) { if (ks == 0) MV0(accA[pr & 1], wo[(pr & 1) * 8 + ks], ob[ks]); else MV1(accA[pr & 1], wo[(pr & 1) * 8 + ks], ob[ks]); SB(); }
        if (pr < 4) { if (ks == 0) MV0(accB[pr & 1], wo[(pr & 1) * 8 + 4 + ks], ob[ks]); else MV1(accB[pr & 1], wo[(pr & 1) * 8 + 4 + ks], ob[ks]); SB(); }
        if (pr == 4 && ks == 0) { NOP(7); SB(); }
        if (pr > 0 && (ks > 1 || pr == 4)) {
          #pragma unroll
          for (int e = (pr == 4 ? ks : (ks - 2) * 2); e < (pr == 4 ? ks + 1 : (ks - 2) * 2 + 2); ++e) { float v = hi ? accB[(pr - 1) & 1][e] : accA[(pr - 1) & 1][e]; o[e] = e ? ((__float_as_uint(v) & ~1u) | tag) : ((__float_as_uint(v) & ~0x1ffu) | tag0); }
          SB(); }
        if (pr < 2) { wo[(pr & 1) * 8 + ks] = wop[((pr + 2) * 8 + ks) * 64]; wo[(pr & 1) * 8 + 4 + ks] = wop[((pr + 2) * 8 + 4 + ks) * 64]; SB(); }
      }
      if (pr > 0) { ST128(o, pcwOff, oPC + ((w * 8 + 2 * (pr - 1)) * 32 + h) * 512); SB(); }
    }
    ST(6); SK(1);
    const bool kv_ready = __all(nfl != EMPTY);
    {
      const unsigned ko = kv_ready ? (unsigned)kOff : OOB, vo = kv_ready ? (unsigned)vOff : OOB;
      #pragma unroll
      for (int t = 0; t < 2; ++t) { kx[t] = LD128(ko, oK + (s + 1) * 262144 + t * 4096); vx[t] = LD128(vo, oVT + (s + 1) * 262144 + t * 1024); }
      asm volatile("s_waitcnt vmcnt(8)" ::: "memory");
    }
    signal_cu(ws, cnt, oFH + s * 8192 + 128, lane);
    {
      ST(7);
      SLEEPN(DELAY_A, (p.role_mask >> 8) & 255);
      u32x4 x[4];
      unsigned spins = 0;
      for (; spins < SPIN_CAP; ++spins) {
        MEMFENCE();
        #pragma unroll
        for (int k = 0; k < 4; ++k) x[k] = LD128(pcrOff + k * 512, oPC + h * 16384);
        unsigned an = 1, orr = 0;
        #pragma unroll
        for (int k = 0; k < 4; ++k) { an &= x[k].x & x[k].y & x[k].z & x[k].w; orr |= x[k].x | x[k].y | x[k].z | x[k].w; }
        bool ok = tag ? (an & 1) : !(orr & 1);
        if (__all(ok)) break;
      }
      if (spins >= SPIN_CAP && lane == 0) ST32(1u, 0u, O_ABT);
      DRAIN();
      ST(8); SK(2);
      u32x4 y[4];
      #pragma unroll
      for (int k = 0; k < 4; ++k) { y[k] = x[k]; y[k].x &= ~0x1ffu; }
      f32x4 a = (__builtin_bit_cast(f32x4, y[0]) + __builtin_bit_cast(f32x4, y[1])) + (__builtin_bit_cast(f32x4, y[2]) + __builtin_bit_cast(f32x4, y[3]));
      #pragma unroll
      for (int e = 0; e < 4; ++e) {
        float v = a[e];
        v += __uint_as_float(DPP8(__float_as_uint(v)));
        a[e] = red_sum(v);
      }
      u32x2 o = {pk(a[0], a[1]), pk(a[2], a[3])};
      if (o.x == EMPTY) o.x = 0xfffefffeu;
      if (o.y == EMPTY) o.y = 0xfffefffeu;
      ST64(o, memOff, oMEM + (s + 1) * 8192 + (h >> 1) * 512 + (h & 1) * 8);
      ST(9); SK(3);
      {
        const unsigned now = ((unsigned)__builtin_amdgcn_s_memrealtime() >> 1);
        unsigned age = min(min((now - (x[0].x >> 1)) & 255u, (now - (x[1].x >> 1)) & 255u), min((now - (x[2].x >> 1)) & 255u, (now - (x[3].x >> 1)) & 255u));
        age = min(age, DPP8(age));
        { auto r = __builtin_amdgcn_permlane32_swap(age, age, false, false); age = min(r[0], r[1]); }
        { auto r = __builtin_amdgcn_permlane16_swap(age, age, false, false); age = min(r[0], r[1]); }
        const int rem = (int)KB_VAL - (int)__builtin_amdgcn_readfirstlane(age);
        nsleep = rem > 0 ? (rem * 3) >> 2 : 0;
      }
      if (!kv_ready) {
        (void)wait_flags(ws, oFH + (s + 1) * 8192 + 4, 2, 4, lane);
        #pragma unroll
        for (int t = 0; t < 2; ++t) { kx[t] = LD128(kOff, oK + (s + 1) * 262144 + t * 4096); vx[t] = LD128(vOff + t * 1024, oVT + (s + 1) * 262144); }
      }
    }
  }
  DRAIN();
  signal_cu(ws, cnt, oFH + 63 * 8192 + 128, lane);
#undef V1
#undef RL
#undef QK1
#undef K2
#undef KA
#undef KB
}

template <bool PERM>
__device__ __forceinline__ void stage_x(char* lx, const u32x4 (&xr)[8], int tid) {
  char* b = lx + (tid & 63) * 528 + (tid >> 6) * 16;
  #pragma unroll
  for (int k = 0; k < 8; ++k) *(u32x4*)(b + (PERM ? (k & 1) * 256 + (k >> 1) * 64 : k * 64)) = xr[k];
}
#define XFRAG(base, kk, rt, tokS) (*(const bf16x8*)((base) + (kk) * 2112 + (rt) * (tokS) * 16))

__device__ __forceinline__ void store_partial(rsrc_t ws, char* tp, const f32x4 (&acc)[8][2], unsigned obase, int w, int lane) {
  const int i = lane & 15, g = lane >> 4;
  char* wp = tp + i * 272 + g * 16;
  const char* rp = tp + g * 272 + i * 16;
  const unsigned so = g * 1024 + w * 256 + i * 16;
  #pragma unroll
  for (int rt = 0; rt < 2; ++rt)
  #pragma unroll
  for (int c = 0; c < 4; ++c) {
    u32x4 o = {pk(acc[2 * c][rt][0], acc[2 * c][rt][1]), pk(acc[2 * c][rt][2], acc[2 * c][rt][3]), pk(acc[2 * c + 1][rt][0], acc[2 * c + 1][rt][1]), pk(acc[2 * c + 1][rt][2], acc[2 * c + 1][rt][3])};
    *(u32x4*)(wp + rt * 4352 + c * 64) = o;
  }
  u32x4 v[8];
  #pragma unroll
  for (int k = 0; k < 8; ++k) v[k] = *(const u32x4*)(rp + k * 1088);
  #pragma unroll
  for (int k = 0; k < 8; ++k) ST128(v[k], so, obase + k * 4096);
}

template <int MM>
__device__ __forceinline__ void role_qkv(const Params& p, rsrc_t ws, char* lds, const int m, const int h) {
  const int tid = threadIdx.x, lane = tid & 63, w = __builtin_amdgcn_readfirstlane(tid >> 6), i = lane & 15, g = lane >> 4;
  int* dec = (int*)(lds + 2 * 34816);
  bf16x8 wf[2][16];
  {
    const bf16x8* src = (const bf16x8*)(p.wsp + O_WQKV) + (size_t)(((h * 4 + w) * 3 + m) * 32) * 64 + lane;
    #pragma unroll
    for (int t = 0; t < 2; ++t)
    #pragma unroll
    for (int kk = 0; kk < 16; ++kk) wf[t][kk] = src[(t * 16 + kk) * 64];
  }
  const int prm = 8 * (i >> 2) + (i & 3);
  constexpr bool ISV = MM == 2;
  constexpr int tokS = 16;
  const char* xb = lds + g * 528 + i * 16;
  const unsigned stOff = ISV ? (unsigned)(w * 2048 + lane * 16) : (unsigned)(w * 1024 + lane * 16);
  int n0 = (p.layer_mask & 1) ? 0 : 64, n1 = (p.layer_mask & 2) ? 0 : 64;
  const unsigned flv = lane < 32 ? (unsigned)lane * 128u : OOB;
  u32x4 xr[8];
  int cur = -1;
  const bool throttle = (p.role_mask & 1) != 0;
  const unsigned l0 = lane == 0 ? 0u : OOB;
  bool credit = false;
  auto poll = [&](unsigned& f1, unsigned& f0, unsigned& fk) {
    f1 = n1 < 64 ? LD32(flv, O_FXN + (64 + n1) * 4096) : EMPTY;
    f0 = n0 < 64 ? LD32(flv, O_FXN + n0 * 4096) : EMPTY;
    fk = (throttle && !credit && n0 >= QKV_AHEAD && n0 < 64) ? LD32(l0, O_FH + ((n0 - QKV_URGENT) * 32 + h) * 256 + 128) : 0u;
  };
  auto decide = [&](unsigned f1, unsigned f0, unsigned fk) -> int { return __all(f1 != EMPTY) ? 64 + n1 : (__all(f0 != EMPTY && fk != EMPTY) ? n0 : -1); };
  auto pick_blocking = [&]() -> int {
    if (w == 0) {
      int d = -2;
      for (unsigned spins = 0; spins < SPIN_CAP; ++spins) {
        MEMFENCE();
        unsigned f1, f0, fk; poll(f1, f0, fk);
        const int dd = decide(f1, f0, fk);
        if (dd >= 0) { d = dd; break; }
        const unsigned long long m1 = __ballot(f1 == EMPTY);
        const unsigned a1 = (n1 < 64 && lane == (int)__builtin_ctzll(m1)) ? flv : OOB;
        const bool w0 = n0 < 64 && __all(f0 != EMPTY);
        bool ab = false;
        for (; spins < SPIN_CAP; ++spins) {
          __builtin_amdgcn_s_sleep(POLL_SLEEP);
          MEMFENCE();
          if (n0 < 64 && !w0) break;
          const unsigned g1 = n1 < 64 ? LD32(a1, O_FXN + (64 + n1) * 4096) : EMPTY;
          const unsigned gk = w0 ? LD32(l0, O_FH + ((n0 - QKV_URGENT) * 32 + h) * 256 + 128) : EMPTY;
          if (__all(g1 != EMPTY) || __all(gk != EMPTY)) break;
          if ((spins & 63u) == 63u) { if (__builtin_amdgcn_readfirstlane(LD32(0u, O_ABT)) == 1u) { ab = true; break; } }
        }
        if (ab) break;
      }
      if (d == -2 && lane == 0) ST32(1u, 0u, O_ABT);
      if (lane == 0) dec[1] = d;
    }
    LBAR();
    return __builtin_amdgcn_readfirstlane(dec[1]);
  };
  auto issue = [&](int item) {
    #pragma unroll
    for (int k = 0; k < 8; ++k) xr[k] = LD128(tid * 16, O_XN + item * 32768 + k * 4096);
    if (item >= 64) { ++n1; credit = true; } else { ++n0; credit = false; }
  };
  if (n0 >= 64 && n1 >= 64) return;
  unsigned* cnt = (unsigned*)(lds + 2 * 34816 + 64);
  if (tid == 0) *cnt = 0u;
  cur = pick_blocking();
  if (cur < 0) return;
  issue(cur);
  const f32x4 z = {0, 0, 0, 0};
  int buf = 0;
#define SQ(k)
  for (;;) {
    unsigned f1 = EMPTY, f0 = EMPTY, fk = EMPTY;
    stage_x<MM != 0>(lds + buf * 34816, xr, tid);
    if (w == 0) poll(f1, f0, fk);
    SQ(0);
    LBAR();
    SQ(1);
    TR(11, cur >> 6, cur & 63, tid == 0 && h == 0 && MM == 1);
    const char* xq = xb + buf * 34816;
    bf16x8 xf[4][2];
    #pragma unroll
    for (int kk = 0; kk < 4; ++kk) { xf[kk][0] = XFRAG(xq, kk, 0, tokS); xf[kk][1] = XFRAG(xq, kk, 1, tokS); }
    SB();
    f32x4 acc[2][2];
    int nxt = -1, nxtv = -1; unsigned nso = 0, nvo = OOB;
    #pragma unroll
    for (int kk = 0; kk < 16; ++kk) {
      #pragma unroll
      for (int t = 0; t < 2; ++t)
      #pragma unroll
      for (int rt = 0; rt < 2; ++rt) {
        if (ISV) acc[t][rt] = MFMA(xf[kk & 3][rt], wf[t][kk], kk ? acc[t][rt] : z);
        else acc[t][rt] = MFMA(wf[t][kk], xf[kk & 3][rt], kk ? acc[t][rt] : z);
      }
      SB();
      if (kk + 4 < 16) { xf[kk & 3][0] = XFRAG(xq, kk + 4, 0, tokS); xf[kk & 3][1] = XFRAG(xq, kk + 4, 1, tokS); }
      if (kk == 6) {
        SQ(2);
        if (w == 0) { int d = decide(f1, f0, fk); if (lane == 0) *dec = d; }
        LBAR();
        SQ(3);
        nxtv = *dec;
      }
      if (kk == 7) { nxt = __builtin_amdgcn_readfirstlane(nxtv); nso = O_XN + (nxt & 127) * 32768; nvo = nxt >= 0 ? (unsigned)tid * 16u : OOB; }
      if (kk >= 8) xr[kk - 8] = LD128(nvo, nso + (kk - 8) * 4096);
      SB();
    }
    SQ(4);
    if (nxt >= 64) { ++n1; credit = true; } else if (nxt >= 0) { ++n0; credit = false; }
    const int s = cur & 63, l = cur >> 6;
    const unsigned ob = O_Q + m * QKV_SZ + (s * 32 + h) * 8192;
    if (ISV) {
      #pragma unroll
      for (int t = 0; t < 2; ++t) {
        u32x4 o = {pk(acc[t][0][0], acc[t][0][1]), pk(acc[t][0][2], acc[t][0][3]), pk(acc[t][1][0], acc[t][1][1]), pk(acc[t][1][2], acc[t][1][3])};
        ST128(o, stOff + t * 1024, ob);
      }
    } else {
      #pragma unroll
      for (int rt = 0; rt < 2; ++rt) {
        u32x4 o = {pk(acc[0][rt][0], acc[0][rt][1]), pk(acc[0][rt][2], acc[0][rt][3]), pk(acc[1][rt][0], acc[1][rt][1]), pk(acc[1][rt][2], acc[1][rt][3])};
        ST128(o, stOff, ob + rt * 4096);
      }
    }
    SQ(5);
    DRAIN();
    SQ(6);
    signal_cu(ws, cnt, O_FH + ((l * 64 + s) * 32 + h) * 256 + m * 4, lane);
    SQ(7);
    TR(2, l, s, tid == 0 && h == 0 && MM == 1);
    if (nxt < 0) {
      if (n0 >= 64 && n1 >= 64) break;
      nxt = pick_blocking();
      if (nxt < 0) break;
      issue(nxt);
    }
    cur = nxt; buf ^= 1;
  }
}

__device__ __forceinline__ void role_att(const Params& p, rsrc_t ws, char* lds, const int l, const int a) {
  const int tid = threadIdx.x, lane = tid & 63, w = __builtin_amdgcn_readfirstlane(tid >> 6), i = lane & 15, g = lane >> 4;
  u32x4* ofx = (u32x4*)lds + lane;
  bf16x8* lwo = (bf16x8*)(lds + 32768 + 4 * 8704) + w * 1024 + lane;
  bf16x8 wo[2][8][4];
  #pragma unroll
  for (int hh = 0; hh < 2; ++hh) {
    const bf16x8* src = (const bf16x8*)(p.wsp + O_WO) + (size_t)((2 * a + hh) * 32 + w * 8) * 256 + lane;
    #pragma unroll
    for (int c = 0; c < 8; ++c)
    #pragma unroll
    for (int ks = 0; ks < 4; ++ks) { wo[hh][c][ks] = src[(c * 4 + ks) * 64]; if (hh == 1 && c >= 4) lwo[((c - 4) * 4 + ks) * 64] = wo[hh][c][ks]; }
    SB();
    #pragma unroll
    for (int c = 0; c < 8; ++c)
    #pragma unroll
    for (int ks = 0; ks < 4; ++ks) { if (hh * 32 + c * 4 + ks < 44) PINA(wo[hh][c][ks]); }
    SB();
  }
  const int hh = w >> 1, qh = w & 1, h = 2 * a + hh;
  const int prm = 8 * (i >> 2) + (i & 3);
  const int tq = 16 * qh + i;
  const float cexp = 0.08838834764831845f * 1.4426950408889634f;
  const f32x4 z = {0, 0, 0, 0};
  const unsigned l16 = lane * 16, kmOff = (g * 8 + (i & 7)) * 16, vmOff = (lane & 31) * 8;
  const unsigned oFH = O_FH + (l * 64 * 32 + h) * 256;
  unsigned* cnt = (unsigned*)(lds + 32768 + 4 * 8704 + 65536);
  if (tid == 0) *cnt = 0u;
  u32x4 qf[4], kf[2][4], vf[8];
  auto issue = [&](int s) {
    const unsigned oq = O_Q + (s * 32 + h) * 8192;
    #pragma unroll
    for (int ks = 0; ks < 4; ++ks) {
      qf[ks] = LD128(l16 + ks * 1024, oq + qh * 4096);
      kf[0][ks] = LD128(l16 + ks * 1024, oq + QKV_SZ);
      kf[1][ks] = LD128(l16 + ks * 1024, oq + QKV_SZ + 4096);
    }
    #pragma unroll
    for (int t8 = 0; t8 < 8; ++t8) vf[t8] = LD128(l16 + (t8 & 3) * 1024, oq + 2 * QKV_SZ + (t8 >> 2) * 4096);
    SB();
  };
  u32x4 kmf[4]; u32x2 vmf[8];
  auto issue_km = [&](int s) {
    const unsigned okm = ((l * 64 + s) * 32 + h) * 2048;
    #pragma unroll
    for (int ks = 0; ks < 4; ++ks) kmf[ks] = LD128(kmOff + ks * 512, O_KM + okm);
    #pragma unroll
    for (int t8 = 0; t8 < 8; ++t8) vmf[t8] = LD64(vmOff + t8 * 256, O_VMT + okm);
    SB();
  };
  const unsigned flv = lane < 3 ? (unsigned)lane * 4u : (lane == 3 ? 128u : OOB);
  if (!wait_flags(ws, oFH, 3, 4, lane)) return;
  issue(0);
  bool early = false;
  for (int s = 0; s < 64; ++s) {
    SA(9);
    f32x4 s0, s1, s2;
    if (!early) {
      #pragma unroll
      for (int ks = 0; ks < 4; ++ks) {
        s0 = MFMA(BC8(kf[0][ks]), BC8(qf[ks]), ks ? s0 : z);
        s1 = MFMA(BC8(kf[1][ks]), BC8(qf[ks]), ks ? s1 : z);
      }
      SB();
      if (!wait_flags(ws, oFH + s * 8192 + 128, 1, 4, lane)) return;
      issue_km(s);
      TR(3, l, s, tid == 0 && a == 0);
    } else {
      #pragma unroll
      for (int ks = 0; ks < 4; ++ks) {
        s0 = MFMA(BC8(kf[0][ks]), BC8(qf[ks]), ks ? s0 : z);
        s1 = MFMA(BC8(kf[1][ks]), BC8(qf[ks]), ks ? s1 : z);
      }
      SB();
    }
    SA(0);
    float sv[12];
    #pragma unroll
    for (int b = 0; b < 4; ++b) {
      sv[b] = (8 * g + b <= tq) ? s0[b] : -INFINITY;
      sv[4 + b] = (8 * g + 4 + b <= tq) ? s1[b] : -INFINITY;
    }
    float mx = fmaxf(fmaxf(fmaxf(sv[0], sv[1]), fmaxf(sv[2], sv[3])), fmaxf(fmaxf(sv[4], sv[5]), fmaxf(sv[6], sv[7])));
    SB();
    #pragma unroll
    for (int ks = 0; ks < 4; ++ks) s2 = MFMA(BC8(kmf[ks]), BC8(qf[ks]), ks ? s2 : z);
    #pragma unroll
    for (int b = 0; b < 4; ++b) { sv[8 + b] = g < 2 ? s2[b] : -INFINITY; mx = fmaxf(mx, sv[8 + b]); }
    SA(1);
    mx = red_max(mx);
    const float nm = -mx * cexp;
    float sum = 0;
    #pragma unroll
    for (int b = 0; b < 12; ++b) { sv[b] = __builtin_amdgcn_exp2f(fmaf(sv[b], cexp, nm)); sum += sv[b]; }
    sum = red_sum(sum);
    const float inv = __builtin_amdgcn_rcpf(sum);
    u32x4 pf = {pk(sv[0] * inv, sv[1] * inv), pk(sv[2] * inv, sv[3] * inv), pk(sv[4] * inv, sv[5] * inv), pk(sv[6] * inv, sv[7] * inv)};
    u32x4 pm = {pk(sv[8] * inv, sv[9] * inv), pk(sv[10] * inv, sv[11] * inv), 0u, 0u};
    SA(2);
    f32x4 oo[8];
    #pragma unroll
    for (int t8 = 0; t8 < 8; ++t8) oo[t8] = MFMA(BC8(vf[t8]), BC8(pf), z);
    #pragma unroll
    for (int t8 = 0; t8 < 8; ++t8) { u32x4 v0 = {vmf[t8].x, vmf[t8].y, 0u, 0u}; oo[t8] = MFMA(BC8(v0), BC8(pm), oo[t8]); }
    u32x4* ofw = ofx + (s & 1) * 1024;
    #pragma unroll
    for (int ks = 0; ks < 4; ++ks) {
      u32x4 o = {pk(oo[2 * ks][0], oo[2 * ks][1]), pk(oo[2 * ks][2], oo[2 * ks][3]), pk(oo[2 * ks + 1][0], oo[2 * ks + 1][1]), pk(oo[2 * ks + 1][2], oo[2 * ks + 1][3])};
      ofw[(w * 4 + ks) * 64] = o;
    }
    SB();
    SA(3);
    if (early) { DRAIN(); signal_cu(ws, cnt, O_FPA + ((l * 64 + s - 1) * 16 + a) * 128, lane); }
    SA(4);
    unsigned nfl = LD32(flv, oFH + ((s + 1) & 63) * 8192);
    LBAR();
    f32x4 acc[8][2];
    SA(5);
    bf16x8 b0 = BC8(ofw[0]), b1 = BC8(ofw[256]);
    bool pre = false;
    unsigned pvo = OOB, kvo = OOB, vvo = OOB;
    const unsigned noq = O_Q + (((s + 1) & 63) * 32 + h) * 8192, nokm = ((l * 64 + ((s + 1) & 63)) * 32 + h) * 2048;
    #pragma unroll
    for (int q = 0; q < 8; ++q) {
      const int h2 = q >> 2, ks = q & 3;
      bf16x8 n0, n1;
      if (q < 7) { n0 = BC8(ofw[((((q + 1) >> 2) * 2 + 0) * 4 + ((q + 1) & 3)) * 64]); n1 = BC8(ofw[((((q + 1) >> 2) * 2 + 1) * 4 + ((q + 1) & 3)) * 64]); }
      if (q == 2) {
        pre = s < 63 && __all(lane >= 3 || nfl != EMPTY); early = pre && __all(nfl != EMPTY);
        pvo = pre ? l16 : OOB; kvo = early ? kmOff : OOB; vvo = early ? vmOff : OOB;
      }
      if (q >= 4) {
        #pragma unroll
        for (int c = 4; c < 8; ++c) wo[1][c][ks] = lwo[((c - 4) * 4 + ks) * 64];
      }
      SB();
      #pragma unroll
      for (int c = 0; c < 8; ++c) {
        acc[c][0] = MFMA(wo[h2][c][ks], b0, q ? acc[c][0] : z);
        acc[c][1] = MFMA(wo[h2][c][ks], b1, q ? acc[c][1] : z);
        const int j = (q - 2) * 8 + c;
        if (q >= 2 && j < 32) {
          SB();
          if (j < 4) qf[j] = LD128(pvo + j * 1024, noq + qh * 4096);
          else if (j < 12) kf[(j - 4) >> 2][j & 3] = LD128(pvo + (j & 3) * 1024, noq + QKV_SZ + ((j - 4) >> 2) * 4096);
          else if (j < 20) vf[j - 12] = LD128(pvo + (j & 3) * 1024, noq + 2 * QKV_SZ + ((j - 12) >> 2) * 4096);
          else if (j < 24) kmf[j - 20] = LD128(kvo + (j - 20) * 512, O_KM + nokm);
          else vmf[j - 24] = LD64(vvo + (j - 24) * 256, O_VMT + nokm);
          SB();
        }
      }
      SB();
      if (q < 7) { b0 = n0; b1 = n1; }
    }
    SA(6);
    store_partial(ws, lds + 32768 + w * 8704, acc, O_PA + (PSLOT(l, s) * 16 + a) * 32768, w, lane);
    TR(4, l, s, tid == 0 && a == 0);
    SA(8);
    if (!early) {
      DRAIN();
      signal_cu(ws, cnt, O_FPA + ((l * 64 + s) * 16 + a) * 128, lane);
      if (s < 63 && !pre) { if (!wait_flags(ws, oFH + (s + 1) * 8192, 3, 4, lane)) return; issue(s + 1); }
    }
  }
}

__device__ __forceinline__ void role_ffn(const Params& p, rsrc_t ws, char* lds, const int l, const int f) {
  const int tid = threadIdx.x, lane = tid & 63, w = __builtin_amdgcn_readfirstlane(tid >> 6), i = lane & 15, g = lane >> 4;
  u32x4* hx = (u32x4*)(lds + 34816) + lane;
  bf16x8* lwd = (bf16x8*)(lds + 34816 + 8192) + w * 1024 + lane;
  bf16x8 wg[2][2][16], wd[4][4];
  {
    const bf16x8* src = (const bf16x8*)(p.wsp + O_WGU) + (size_t)((f * 4 + w) * 64) * 64 + lane;
    #pragma unroll
    for (int m = 0; m < 2; ++m)
    #pragma unroll
    for (int t = 0; t < 2; ++t) {
      #pragma unroll
      for (int kk = 0; kk < 16; ++kk) wg[m][t][kk] = src[((m * 2 + t) * 16 + kk) * 64];
      SB();
      #pragma unroll
      for (int kk = 0; kk < 16; ++kk) { if ((m * 2 + t) * 16 + kk < 44) PINA(wg[m][t][kk]); }
      SB();
    }
    const bf16x8* sd = (const bf16x8*)(p.wsp + O_WD) + (size_t)(f * 32 + w * 8) * 256 + lane;
    #pragma unroll
    for (int c = 0; c < 4; ++c)
    #pragma unroll
    for (int ks = 0; ks < 4; ++ks) { wd[c][ks] = sd[(c * 4 + ks) * 64]; lwd[(c * 4 + ks) * 64] = sd[((c + 4) * 4 + ks) * 64]; }
  }
  const f32x4 z = {0, 0, 0, 0};
  const char* xb = lds + g * 528 + i * 16;
  const unsigned flv = lane < 32 ? (unsigned)lane * 128u : OOB;
  unsigned* cnt = (unsigned*)(lds + 34816 + 8192 + 65536 + 4 * 8704);
  if (tid == 0) *cnt = 0u;
  u32x4 xr[8];
  auto issue = [&](int s) {
    #pragma unroll
    for (int k = 0; k < 8; ++k) xr[k] = LD128(tid * 16, O_XN1 + (l * 64 + s) * 32768 + k * 4096);
    SB();
  };
  bool early = false;
  float hprev = 0.f;
  for (int s = 0; s < 64; ++s) {
    SF(9);
    if (!early) { if (!wait_flags(ws, O_FXN1 + (l * 64 + s) * 4096, 32, 128, lane)) return; issue(s); }
    SF(0);
    stage_x<false>(lds, xr, tid);
    SF(1);
    LBAR();
    SF(2);
    TR(6, l, s, tid == 0 && f == 0);
    f32x4 ag[2][2][2];
    auto gelu1 = [&](int rt, int t, int b) -> float {
      const float gv = ag[0][t][rt][b], uv = ag[1][t][rt][b];
      const float y2 = gv * fmaf(gv * gv, -0.044715f * 2.302208198144325f, -2.302208198144325f);
      return gv * __builtin_amdgcn_rcpf(1.0f + __builtin_amdgcn_exp2f(y2)) * uv;
    };
    u32x4 ho;
    bf16x8 xf[4];
    #pragma unroll
    for (int kk = 0; kk < 4; ++kk) xf[kk] = XFRAG(xb, kk, 0, 16);
    SB();
    #pragma unroll
    for (int j = 0; j < 32; ++j) {
      const int rt = j >> 4, kk = j & 15;
      #pragma unroll
      for (int m = 0; m < 2; ++m) {
        #pragma unroll
        for (int t = 0; t < 2; ++t) ag[m][t][rt] = MFMA(wg[m][t][kk], xf[j & 3], kk ? ag[m][t][rt] : z);
        SB();
        if (rt == 1 && kk >= 2 && kk < 10 && m == 0) {
          const int e = kk - 2; float hv = gelu1(0, e >> 2, e & 3);
          if (e & 1) { ho[e >> 1] = pk(hprev, hv); } else hprev = hv;
          SB();
        }
      }
      if (j + 4 < 32) xf[j & 3] = XFRAG(xb, (j + 4) & 15, (j + 4) >> 4, 16);
      if (j == 6 && early) { DRAIN(); signal_cu(ws, cnt, O_FPF + ((l * 64 + s - 1) * 16 + f) * 128, lane); }
      if (j == 27) hx[w * 64] = ho;
      SB();
    }
    SF(3);
    bf16x8 wl[4];
    #pragma unroll
    for (int c = 0; c < 4; ++c) wl[c] = lwd[(c * 4) * 64];
    unsigned nfl = LD32(flv, O_FXN1 + (l * 64 + ((s + 1) & 63)) * 4096);
    LBAR();
    SF(4);
    f32x4 acc[8][2];
    unsigned nvo = OOB; const unsigned nso = O_XN1 + (l * 64 + ((s + 1) & 63)) * 32768;
    #pragma unroll
    for (int rt = 0; rt < 2; ++rt) {
      bf16x8 hb[4];
      #pragma unroll
      for (int ks = 0; ks < 4; ++ks) hb[ks] = BC8(hx[(rt * 4 + ks) * 64]);
      SB();
      #pragma unroll
      for (int ks = 0; ks < 4; ++ks) {
        bf16x8 wn[4];
        #pragma unroll
        for (int c = 0; c < 4; ++c) wn[c] = lwd[(c * 4 + ((ks + 1) & 3)) * 64];
        SB();
        #pragma unroll
        for (int c = 0; c < 8; ++c) {
          acc[c][rt] = MFMA(c < 4 ? wd[c & 3][ks] : wl[c & 3], hb[ks], ks ? acc[c][rt] : z);
          if (rt == 0 && (c & 3) == 1) {
            SB();
            const int e = ks * 2 + (c >> 2); float hv = gelu1(1, e >> 2, e & 3);
            if (e & 1) { ho[e >> 1] = pk(hprev, hv); } else hprev = hv;
            SB();
          }
          if (rt == 1 && (c & 3) == 1) { SB(); xr[ks * 2 + (c >> 2)] = LD128(nvo, nso + (ks * 2 + (c >> 2)) * 4096); SB(); }
        }
        SB();
        #pragma unroll
        for (int c = 0; c < 4; ++c) wl[c] = wn[c];
      }
      if (rt == 0) {
        hx[(4 + w) * 64] = ho;
        early = s < 63 && __all(nfl != EMPTY);
        nvo = early ? (unsigned)tid * 16u : OOB;
        LBAR();
        SF(5);
      }
    }
    SF(6);
    store_partial(ws, lds + 34816 + 8192 + 65536 + w * 8704, acc, O_PF + (PSLOT(l, s) * 14 + f) * 32768, w, lane);
    TR(7, l, s, tid == 0 && f == 0);
    SF(8);
    if (!early) { DRAIN(); signal_cu(ws, cnt, O_FPF + ((l * 64 + s) * 16 + f) * 128, lane); }
  }
}

__device__ __forceinline__ u32x4 ln_row(const float (&v)[8]) {
  float s = ((v[0] + v[1]) + (v[2] + v[3])) + ((v[4] + v[5]) + (v[6] + v[7]));
  const float mean = wave_sum(s) * (1.0f / 512.0f);
  float q = 0;
  #pragma unroll
  for (int j = 0; j < 8; ++j) { float d = v[j] - mean; q += d * d; }
  const float var = wave_sum(q) * (1.0f / 512.0f);
  const float mb = rb(mean), rden = __builtin_amdgcn_rcpf(rb(__builtin_amdgcn_sqrtf(rb(rb(var) + 1e-5f))));
  float y[8];
  #pragma unroll
  for (int j = 0; j < 8; ++j) y[j] = rb(v[j] - mb) * rden;
  u32x4 o = {pk(y[0], y[1]), pk(y[2], y[3]), pk(y[4], y[5]), pk(y[6], y[7])};
  return o;
}
__device__ __forceinline__ void role_red(const Params& p, rsrc_t ws, char* lds, const int r) {
  const int tid = threadIdx.x, lane = tid & 63, w = __builtin_amdgcn_readfirstlane(tid >> 6);
  u32x4* xres = (u32x4*)lds + lane;
  if (p.role_mask & 32) {
    u32x4 xr[16];
    #pragma unroll
    for (int k = 0; k < 16; ++k) xr[k] = *(const u32x4*)(p.x + ((size_t)((k * 4 + w) * 32 + r) * 512 + lane * 8));
    #pragma unroll
    for (int k = 0; k < 16; ++k) {
      const int s = k * 4 + w;
      float v[8] = {lo16(xr[k].x), hi16(xr[k].x), lo16(xr[k].y), hi16(xr[k].y), lo16(xr[k].z), hi16(xr[k].z), lo16(xr[k].w), hi16(xr[k].w)};
      ST128(ln_row(v), lane * 16, O_XN + s * 32768 + r * 1024);
      DRAIN();
      if (lane == 0) ST32(1u, r * 128, O_FXN + s * 4096);
    }
  }
  const int st = w & 1, l = w >> 1;
  if (!(p.layer_mask & (1 << l)) || !(p.role_mask & (st ? 16 : 8))) return;
  const int np = st ? 14 : 16;
  for (int s = 0; s < 64; ++s) {
    u32x4 xv;
    if (w == 0) xv = *(const u32x4*)(p.x + ((size_t)(s * 32 + r) * 512 + lane * 8));
    if (!wait_flags(ws, (st ? O_FPF : O_FPA) + (l * 64 + s) * 2048, np, 128, lane)) return;
    TR(st ? 10 : 9, l, s, lane == 0 && r == 0);
    const unsigned ob = (st ? O_PF + PSLOT(l, s) * 14 * 32768 : O_PA + PSLOT(l, s) * 16 * 32768) + r * 1024;
    u32x4 a[16];
    #pragma unroll
    for (int k = 0; k < 16; ++k) { if (k < 14 || !st) a[k] = LD128(lane * 16, ob + k * 32768); else a[k] = (u32x4){0u, 0u, 0u, 0u}; }
    if (w > 0) xv = xres[s * 64];
    f32x4 sa = {0, 0, 0, 0}, sb = {0, 0, 0, 0};
    #pragma unroll
    for (int k = 0; k < 16; ++k) {
      sa += (f32x4){lo16(a[k].x), hi16(a[k].x), lo16(a[k].y), hi16(a[k].y)};
      sb += (f32x4){lo16(a[k].z), hi16(a[k].z), lo16(a[k].w), hi16(a[k].w)};
    }
    float v[8];
    v[0] = rb(lo16(xv.x) + rb(sa[0])); v[1] = rb(hi16(xv.x) + rb(sa[1])); v[2] = rb(lo16(xv.y) + rb(sa[2])); v[3] = rb(hi16(xv.y) + rb(sa[3]));
    v[4] = rb(lo16(xv.z) + rb(sb[0])); v[5] = rb(hi16(xv.z) + rb(sb[1])); v[6] = rb(lo16(xv.w) + rb(sb[2])); v[7] = rb(hi16(xv.w) + rb(sb[3]));
    u32x4 xo = {pk(v[0], v[1]), pk(v[2], v[3]), pk(v[4], v[5]), pk(v[6], v[7])};
    if (w == 3) {
      *(u32x4*)(p.out + ((size_t)(s * 32 + r) * 512 + lane * 8)) = xo;
    } else {
      xres[s * 64] = xo;
      const unsigned od = st ? O_XN + (64 + s) * 32768 : O_XN1 + (l * 64 + s) * 32768;
      ST128(ln_row(v), lane * 16, od + r * 1024);
      asm volatile("s_waitcnt vmcnt(0) lgkmcnt(0)" ::: "memory");
      if (lane == 0) ST32(1u, r * 128, st ? O_FXN + (64 + s) * 4096 : O_FXN1 + (l * 64 + s) * 4096);
    }
    TR(st ? 8 : 5, l, s, lane == 0 && r == 0);
    TR(12, l, s, lane == 0 && r == 17 && st);
    TR(13, l, s, lane == 0 && r == 31 && st);
  }
}

__global__ void __launch_bounds__(256, 1) k_main(Params p) {
  extern __shared__ __attribute__((aligned(16))) char lds[];
  rsrc_t ws = __builtin_amdgcn_make_buffer_rsrc(p.wsp, 0, 256u * 1024 * 1024, 0x00020000);
#define MAPMODE 0
  int b = blockIdx.x;
  {
    const int x = b & 7, y = b >> 3;
    if (MAPMODE == 1) b = x < 2 ? x * 32 + y : 64 + y * 6 + (x - 2);
    if (MAPMODE == 2) b = y < 16 ? (x < 4 ? (x >> 1) * 32 + (x & 1) * 16 + y : 64 + y * 4 + (x - 4)) : 128 + (y - 16) * 8 + x;
    if (MAPMODE == 3) b = y < 8 ? (x >> 2) * 32 + (x & 3) * 8 + y : b;
  }
  TR(15, 0, 0, b == 0 && threadIdx.x == 0);
  if (b < 64) { if ((p.role_mask & 1) && (p.layer_mask & (1 << (b >> 5)))) role_chain(p, ws, lds, b >> 5, b & 31); }
  else if (b < 96) { if (p.role_mask & 2) role_qkv<2>(p, ws, lds, 2, b & 31); }
  else if (b < 128) { if (p.role_mask & 2) role_qkv<1>(p, ws, lds, 1, b & 31); }
  else if (b < 160) { if (p.role_mask & 2) role_qkv<0>(p, ws, lds, 0, b & 31); }
  else if (b < 192) { if ((p.role_mask & 4) && (p.layer_mask & (1 << ((b - 160) >> 4)))) role_att(p, ws, lds, (b - 160) >> 4, b & 15); }
  else if (b < 220) { const int l = (b - 192) >= 14; if ((p.role_mask & 64) && (p.layer_mask & (1 << l))) role_ffn(p, ws, lds, l, b - 192 - 14 * l); }
  else if (b < 252) { if (p.role_mask & (8 | 16 | 32)) role_red(p, ws, lds, b - 220); }
}

extern "C" void kernel_launch(void* const* d_in, const int* in_sizes, int n_in, void* d_out, int out_size, void* d_ws, size_t ws_size, hipStream_t stream) {
  const u16* x = (const u16*)d_in[0]; const u16* mem0 = (const u16*)d_in[1];
  const u16* Wq = (const u16*)d_in[2]; const u16* Wk = (const u16*)d_in[3]; const u16* Wv = (const u16*)d_in[4]; const u16* Wo = (const u16*)d_in[5];
  const u16* wg = (const u16*)d_in[8]; const u16* wu = (const u16*)d_in[9]; const u16* wd = (const u16*)d_in[10];
  (void)hipFuncSetAttribute((const void*)k_main, hipFuncAttributeMaxDynamicSharedMemorySize, LDS_BYTES);
  k_prep<<<PREP_B + INIT_B, 256, 0, stream>>>(Wq, Wk, Wv, Wo, wg, wu, wd, (char*)d_ws);
  Params p{x, mem0, (u16*)d_out, (char*)d_ws, 127 | g_delay, 3};
  k_main<<<256, 256, LDS_BYTES, stream>>>(p);
}
```

```cpp
#include <hip/hip_runtime.h>
#include <hip/hip_bf16.h>
#include <stdint.h>

typedef unsigned short u16;
typedef short bf16x8 __attribute__((ext_vector_type(8)));
typedef float f32x4 __attribute__((ext_vector_type(4)));
typedef float f32x2 __attribute__((ext_vector_type(2)));
typedef unsigned u32x4 __attribute__((ext_vector_type(4)));
typedef unsigned u32x2 __attribute__((ext_vector_type(2)));
typedef __bf16 bfx2 __attribute__((ext_vector_type(2)));

constexpr unsigned O_FXN  = 0;
constexpr unsigned O_FXN1 = O_FXN + 524288;
constexpr unsigned O_FPA  = O_FXN1 + 524288;
constexpr unsigned O_FPF  = O_FPA + 262144;
constexpr unsigned O_ABT  = O_FPF + 262144;
constexpr unsigned O_FH   = O_ABT + 256;
constexpr unsigned O_MEM  = O_FH + 1048576;
constexpr unsigned O_PC   = O_MEM + 1048576;
#define PCRING 8
constexpr unsigned CTRL_BYTES = (O_PC + PCRING * 1048576 + 16383) & ~16383u;
constexpr unsigned O_WQKV = (CTRL_BYTES + 4095) & ~4095u;
constexpr unsigned O_WO   = O_WQKV + 3 * 512 * 4096 * 2;
constexpr unsigned O_WGU  = O_WO + 4096 * 512 * 2;
constexpr unsigned O_WD   = O_WGU + 2 * 512 * 1792 * 2;
constexpr unsigned O_XN   = O_WD + 1792 * 512 * 2;
constexpr unsigned O_XN1  = O_XN + 2 * 64 * 32768;
constexpr unsigned O_Q    = O_XN1 + 2 * 64 * 32768;
constexpr unsigned QKV_SZ = 64 * 32 * 8192;
constexpr unsigned O_KM   = O_Q + 3 * QKV_SZ;
constexpr unsigned O_VMT  = O_KM + 2 * 64 * 32 * 2048;
constexpr unsigned O_PA   = O_VMT + 2 * 64 * 32 * 2048;
constexpr unsigned O_PF   = O_PA + 64u * 16 * 32768;
constexpr unsigned WS_END = O_PF + 64u * 14 * 32768;
static_assert(WS_END <= 256u * 1024 * 1024, "workspace");

constexpr int LDS_BYTES = 160 * 1024;
#define SC1 16
#define OOB 0x7fffff00u
#define EMPTY 0xffffffffu
#define SPIN_CAP (1u << 20)
#define DELAY_A 9
#define DELAY_B 151

#define QKV_AHEAD 20
#define QKV_URGENT 8
#define SIGPOS 0
#define WOPRE 2
#define POLL_SLEEP 4
#define KB_VAL DELAY_B
#define SLEEPN(c, r) __builtin_amdgcn_s_sleep(c)
#define g_delay 0
#define PSLOT(l, s) (s)
#define SB() __builtin_amdgcn_sched_barrier(0)
#define MFMA(a, b, c) __builtin_amdgcn_mfma_f32_16x16x32_bf16(a, b, c, 0, 0, 0)
#define BC8(x) __builtin_bit_cast(bf16x8, x)
#define MA0(acc, A, B) asm volatile("v_mfma_f32_16x16x32_bf16 %0, %1, %2, 0" : "=v"(acc) : "a"(A), "v"(B))
#define MA1(acc, A, B) asm volatile("v_mfma_f32_16x16x32_bf16 %0, %1, %2, %0" : "+v"(acc) : "a"(A), "v"(B))
#define MV0(acc, A, B) asm volatile("v_mfma_f32_16x16x32_bf16 %0, %1, %2, 0" : "=v"(acc) : "v"(A), "v"(B))
#define MV1(acc, A, B) asm volatile("v_mfma_f32_16x16x32_bf16 %0, %1, %2, %0" : "+v"(acc) : "v"(A), "v"(B))
#define NOP(n) asm volatile("s_nop %0" :: "i"(n))
#define PINA(x) asm volatile("" : "+a"(x))
#define PINV(x) asm volatile("" : "+v"(x))
#define DPP8(x) ((unsigned)__builtin_amdgcn_update_dpp(0, (int)(x), 0x128, 0xf, 0xf, false))
#define DRAIN() asm volatile("s_waitcnt vmcnt(0)" ::: "memory")
#define MEMFENCE() asm volatile("" ::: "memory")
#define LBAR() do { asm volatile("s_waitcnt lgkmcnt(0)" ::: "memory"); __builtin_amdgcn_s_barrier(); asm volatile("" ::: "memory"); } while (0)
#define LD128(off, so) __builtin_amdgcn_raw_buffer_load_b128(ws, (off), (so), SC1)
#define LD64(off, so) __builtin_amdgcn_raw_buffer_load_b64(ws, (off), (so), SC1)
#define LD32(off, so) __builtin_amdgcn_raw_buffer_load_b32(ws, (off), (so), SC1)
#define ST128(v, off, so) __builtin_amdgcn_raw_buffer_store_b128((v), ws, (off), (so), SC1)
#define ST64(v, off, so) __builtin_amdgcn_raw_buffer_store_b64((v), ws, (off), (so), SC1)
#define ST32(v, off, so) __builtin_amdgcn_raw_buffer_store_b32((v), ws, (off), (so), SC1)
typedef __amdgpu_buffer_rsrc_t rsrc_t;

__device__ __forceinline__ unsigned pk(float a, float b) { f32x2 v = {a, b}; bfx2 r = __builtin_convertvector(v, bfx2); return __builtin_bit_cast(unsigned, r); }
__device__ __forceinline__ float rb(float a) { return __uint_as_float(pk(a, a) << 16); }
__device__ __forceinline__ float lo16(unsigned u) { return __uint_as_float(u << 16); }
__device__ __forceinline__ float hi16(unsigned u) { return __uint_as_float(u & 0xffff0000u); }
__device__ __forceinline__ float red_max(float x) {
  auto r = __builtin_amdgcn_permlane32_swap(__float_as_uint(x), __float_as_uint(x), false, false);
  x = fmaxf(__uint_as_float(r[0]), __uint_as_float(r[1]));
  auto q = __builtin_amdgcn_permlane16_swap(__float_as_uint(x), __float_as_uint(x), false, false);
  return fmaxf(__uint_as_float(q[0]), __uint_as_float(q[1]));
}
__device__ __forceinline__ float red_sum(float x) {
  auto r = __builtin_amdgcn_permlane32_swap(__float_as_uint(x), __float_as_uint(x), false, false);
  x = __uint_as_float(r[0]) + __uint_as_float(r[1]);
  auto q = __builtin_amdgcn_permlane16_swap(__float_as_uint(x), __float_as_uint(x), false, false);
  return __uint_as_float(q[0]) + __uint_as_float(q[1]);
}
__device__ __forceinline__ float wave_sum(float x) {
  x += __uint_as_float((unsigned)__builtin_amdgcn_update_dpp(0, (int)__float_as_uint(x), 0x121, 0xf, 0xf, false));
  x += __uint_as_float((unsigned)__builtin_amdgcn_update_dpp(0, (int)__float_as_uint(x), 0x122, 0xf, 0xf, false));
  x += __uint_as_float((unsigned)__builtin_amdgcn_update_dpp(0, (int)__float_as_uint(x), 0x124, 0xf, 0xf, false));
  x += __uint_as_float(DPP8(__float_as_uint(x)));
  return red_sum(x);
}

__device__ __forceinline__ bool wait_flags(rsrc_t ws, unsigned off, int n, unsigned stride, int lane) {
  unsigned vo = lane < n ? (unsigned)lane * stride : OOB;
  for (unsigned spins = 0; spins < SPIN_CAP; ++spins) {
    MEMFENCE();
    unsigned v = LD32(vo, off);
    unsigned long long miss = __ballot(v == EMPTY);
    if (!miss) return true;
    const int fm = __builtin_ctzll(miss);
    const unsigned v1 = lane == fm ? vo : OOB;
    if (v != EMPTY) vo = OOB;
    for (; spins < SPIN_CAP; ++spins) {
      __builtin_amdgcn_s_sleep(POLL_SLEEP);
      MEMFENCE();
      if (__all(LD32(v1, off) != EMPTY)) break;
      if ((spins & 63u) == 63u) { if (__builtin_amdgcn_readfirstlane(LD32(0u, O_ABT)) == 1u) return false; }
    }
    if (lane == fm) vo = OOB;
  }
  if (lane == 0) ST32(1u, 0u, O_ABT);
  return false;
}

__device__ __forceinline__ void signal_cu(rsrc_t ws, unsigned* cnt, unsigned off, int lane) {
  if (lane == 0) { unsigned old = atomicAdd(cnt, 1u); if ((old & 3u) == 3u) ST32(1u, 0u, off); }
}
struct Params {
  const u16* x; const u16* mem0; u16* out; char* wsp; int role_mask; int layer_mask;
};

__device__ __forceinline__ u32x4 ln_row(const float (&v)[8]) {
  float s = ((v[0] + v[1]) + (v[2] + v[3])) + ((v[4] + v[5]) + (v[6] + v[7]));
  const float mean = wave_sum(s) * (1.0f / 512.0f);
  float q = 0;
  #pragma unroll
  for (int j = 0; j < 8; ++j) { float d = v[j] - mean; q += d * d; }
  const float var = wave_sum(q) * (1.0f / 512.0f);
  const float mb = rb(mean), rden = __builtin_amdgcn_rcpf(rb(__builtin_amdgcn_sqrtf(rb(rb(var) + 1e-5f))));
  float y[8];
  #pragma unroll
  for (int j = 0; j < 8; ++j) y[j] = rb(v[j] - mb) * rden;
  u32x4 o = {pk(y[0], y[1]), pk(y[2], y[3]), pk(y[4], y[5]), pk(y[6], y[7])};
  return o;
}

constexpr int PB1 = 3 * 16 * 8, PB2 = 128, PB3 = 2 * 16 * 4, PB4 = 56;
constexpr int PREP_B = PB1 + PB2 + PB3 + PB4, LN_B = 512, INIT_B = CTRL_BYTES / 16384 + LN_B;
__global__ void __launch_bounds__(256) k_prep(const u16* x, const u16* Wq, const u16* Wk, const u16* Wv, const u16* Wo, const u16* wg, const u16* wu, const u16* wd, char* wsp) {
  __shared__ u32x4 t[2048];
  int b = blockIdx.x; const int tid = threadIdx.x;
  if (b >= PREP_B + INIT_B - LN_B) {
    const int row = (b - (PREP_B + INIT_B - LN_B)) * 4 + (tid >> 6), lane = tid & 63;
    const u32x4 xr = *(const u32x4*)(x + (size_t)row * 512 + lane * 8);
    float v[8] = {lo16(xr.x), hi16(xr.x), lo16(xr.y), hi16(xr.y), lo16(xr.z), hi16(xr.z), lo16(xr.w), hi16(xr.w)};
    *(u32x4*)(wsp + O_XN + (size_t)row * 1024 + lane * 16) = ln_row(v);
    return;
  }
  if (b >= PREP_B) {
    const u32x4 e = {EMPTY, EMPTY, EMPTY, EMPTY};
    #pragma unroll
    for (int k = 0; k < 4; ++k) ((u32x4*)wsp)[(b - PREP_B) * 1024 + k * 256 + tid] = e;
    return;
  }
  const u16* src; int ld, kk, np, nwin = 16; char* dst; int wstride, tstride;
  if (b < PB1) {
    const int m = b >> 7, id = b & 127; kk = id >> 3; np = id & 7;
    src = m == 0 ? Wq : (m == 1 ? Wk : Wv); ld = 4096;
    dst = wsp + O_WQKV + (size_t)((np * 16 * 3 + m) * 32 + kk) * 1024; wstride = 3 * 32768; tstride = 16384;
  } else if (b < PB1 + PB2) {
    kk = b - PB1; np = 0; src = Wo; ld = 512;
    dst = wsp + O_WO + (size_t)((kk >> 2) * 128 + (kk & 3)) * 1024; wstride = 8192; tstride = 4096;
  } else if (b < PB1 + PB2 + PB3) {
    const int id = b - PB1 - PB2; const int m = id >> 6; kk = (id >> 2) & 15; np = id & 3;
    src = m == 0 ? wg : wu; ld = 1792; if (np == 3) nwin = 8;
    dst = wsp + O_WGU + (size_t)((np * 16 * 2 + m) * 32 + kk) * 1024; wstride = 2 * 32768; tstride = 16384;
  } else {
    kk = b - PB1 - PB2 - PB3; np = 0; src = wd; ld = 512;
    dst = wsp + O_WD + (size_t)((kk >> 2) * 128 + (kk & 3)) * 1024; wstride = 8192; tstride = 4096;
  }
  const int g = tid >> 6, n8 = tid & 63;
  if ((n8 >> 2) < nwin) {
    u32x4 r[8];
    #pragma unroll
    for (int j = 0; j < 8; ++j) r[j] = *(const u32x4*)(src + (size_t)(kk * 32 + g * 8 + j) * ld + np * 512 + n8 * 8);
    #pragma unroll
    for (int c = 0; c < 8; ++c) {
      u32x4 o;
      #pragma unroll
      for (int q = 0; q < 4; ++q) {
        const unsigned lo = r[2 * q][c >> 1], hi = r[2 * q + 1][c >> 1];
        o[q] = (c & 1) ? ((lo >> 16) | (hi & 0xffff0000u)) : ((lo & 0xffffu) | (hi << 16));
      }
      t[((n8 >> 2) * 2 + (c >> 2)) * 64 + g * 16 + (n8 & 3) * 4 + (c & 3)] = o;
    }
  }
  __syncthreads();
  #pragma unroll
  for (int k = 0; k < 8; ++k) {
    const int f = k * 4 + g;
    if ((f >> 1) < nwin) *(u32x4*)(dst + (size_t)(f >> 1) * wstride + (f & 1) * tstride + n8 * 16) = t[f * 64 + n8];
  }
}

__device__ __forceinline__ void role_chain(const Params& p, rsrc_t ws, char* lds, const int l, const int h) {
  const int tid = threadIdx.x, lane = tid & 63, w = __builtin_amdgcn_readfirstlane(tid >> 6), i = lane & 15, g = lane >> 4;
  const int vi = g * 8 + (i & 7);
  bf16x8* ldsWo = (bf16x8*)lds;
  bf16x8* memf = (bf16x8*)(lds + 131072);
  f32x4* sc = (f32x4*)(lds + 131072 + 8192);
  bf16x8* of = (bf16x8*)(lds + 131072 + 8192 + 7168);
  bf16x8* wx = (bf16x8*)(lds + 131072 + 8192 + 7168 + 2048) + w * 192 + lane;
  unsigned* cnt = (unsigned*)(lds + 131072 + 8192 + 7168 + 2048 + 12288);
  bf16x8 wf[3][2][16];
  {
    const bf16x8* src = (const bf16x8*)(p.wsp + O_WQKV) + (size_t)((h * 4 + w) * 96) * 64 + lane;
    const bf16x8* so = (const bf16x8*)(p.wsp + O_WO) + (size_t)h * 8192 + tid;
    #pragma unroll
    for (int m = 0; m < 3; ++m) {
      #pragma unroll
      for (int t = 0; t < 2; ++t)
      #pragma unroll
      for (int kk = 0; kk < 16; ++kk) { wf[m][t][kk] = src[((m * 2 + t) * 16 + kk) * 64]; }
      bf16x8 tw[16];
      if (m < 2) {
        #pragma unroll
        for (int k = 0; k < 16; ++k) tw[k] = so[(m * 16 + k) * 256];
      }
      SB();
      if (m == 2) { wx[0] = wf[2][0][14]; wx[64] = wf[2][0][15]; wx[128] = wf[2][1][14]; }
      if (m < 2) {
        #pragma unroll
        for (int t = 0; t < 2; ++t)
        #pragma unroll
        for (int kk = 0; kk < 16; ++kk) PINA(wf[m][t][kk]);
        #pragma unroll
        for (int k = 0; k < 16; ++k) ldsWo[(m * 16 + k) * 256 + tid] = tw[k];
      }
      SB();
    }
  }
  rsrc_t rM0 = __builtin_amdgcn_make_buffer_rsrc((void*)p.mem0, 0, 8192, 0x00020000);
  const int prm = 8 * (i >> 2) + (i & 3);
  const int kOff = w * 1024 + lane * 16;
  const int vOff = w * 2048 + lane * 16;
  const unsigned oK = O_Q + QKV_SZ + h * 8192, oVT = O_Q + 2 * QKV_SZ + h * 8192;
  const unsigned oFH = O_FH + (l * 64 * 32 + h) * 256;
  const unsigned oKM = O_KM + (l * 64 * 32 + h) * 2048, oVMT = O_VMT + (l * 64 * 32 + h) * 2048;
  const unsigned oMEM = O_MEM + l * 64 * 8192, oPC0 = O_PC + l * 32 * 32 * 512;
  u32x4 kx[2], vx[2];
  if (tid == 0) *cnt = 0u;
  if (!wait_flags(ws, oFH + 4, 2, 4, lane)) return;
  #pragma unroll
  for (int t = 0; t < 2; ++t) { kx[t] = LD128(kOff, oK + t * 4096); vx[t] = LD128(vOff + t * 1024, oVT); }
  __syncthreads();
  const int hi = i >> 3;
  const bf16x8* mfp = memf + g * 8 + (i & 7);
  const bf16x8* wop = ldsWo + w * 2048 + lane;
  const f32x4* scx = sc + hi * 32 + vi;
  const f32x2* scm = (const f32x2*)(sc + 64 + vi) + hi;
  f32x4* scw = sc + w * 96 + vi;
  u32x4* ofw = hi ? (u32x4*)(sc) + 384 + lane : (u32x4*)of + w * 32 + vi;
  const bf16x8* ofr_p = mfp + 960;
  const int tid16 = tid * 16;
  const unsigned pcwOff = vi * 16 + hi * 16384;
  const unsigned kmOff = hi ? OOB : (unsigned)((w * 32 + vi) * 16);
  const unsigned vmOff = g < 2 ? (unsigned)(w * 512 + lane * 8) : OOB;
  const int pcrOff = (lane >> 3) * 2048 + (w * 8 + (lane & 7)) * 16;
  const unsigned memOff = lane < 8 ? (unsigned)(w * 128 + lane * 16) : OOB;
  const unsigned flOff = lane < 2 ? (unsigned)(lane * 4 + 4) : OOB;
  const float cexp = 0.08838834764831845f * 1.4426950408889634f;
  int nsleep = 0;
  for (int s = 0; s < 64; ++s) {
    {
      u32x4 x[2];
      if (s == 0) {
        #pragma unroll
        for (int k = 0; k < 2; ++k) { int c = k * 256 + tid; x[k] = __builtin_amdgcn_raw_buffer_load_b128(rM0, ((c & 7) * 512 + (c >> 3) * 8) * 2, 0, 0); }
      } else {
        for (int d_ = 0; d_ < nsleep; ++d_) __builtin_amdgcn_s_sleep(1);
        unsigned spins = 0;
        for (; spins < SPIN_CAP; ++spins) {
          MEMFENCE();
          #pragma unroll
          for (int k = 0; k < 2; ++k) x[k] = LD128(tid16, oMEM + s * 8192 + k * 4096);
          bool ok = true;
          #pragma unroll
          for (int k = 0; k < 2; ++k) ok &= (x[k].x != EMPTY) && (x[k].y != EMPTY) && (x[k].z != EMPTY) && (x[k].w != EMPTY);
          if (__all(ok)) break;
        }
        if (spins >= SPIN_CAP && lane == 0) ST32(1u, 0u, O_ABT);
      }
      #pragma unroll
      for (int k = 0; k < 2; ++k) ((u32x4*)memf)[tid + k * 256] = x[k];
    }
    const unsigned tstamp = ((unsigned)__builtin_amdgcn_s_memrealtime() >> 1) & 255u;
    LBAR();
    f32x4 aq[2], ak[2], av[2];
    bf16x8 wo[16];
    bf16x8 mf[16];
    #pragma unroll
    for (int kk = 0; kk < 16; ++kk) mf[kk] = mfp[kk * 32];
    SB();
#define QK1(acc, m, kk, t) do { if ((kk) == 0) MA0(acc[t], wf[m][t][kk], mf[kk]); else MA1(acc[t], wf[m][t][kk], mf[kk]); SB(); } while (0)
#define V1(kk, t) do { if ((kk) == 0) MV0(av[t], mf[kk], wf[2][t][kk]); else MV1(av[t], mf[kk], wf[2][t][kk]); SB(); if ((t) == 1 && (kk) >= WOPRE) { wo[(kk) - WOPRE] = wop[((kk) - WOPRE) * 64]; SB(); } } while (0)
#define RL(kk)
#define K2(kk) do { QK1(ak, 1, kk, 0); QK1(ak, 1, kk, 1); RL(kk); } while (0)
    #pragma unroll
    for (int kk = 0; kk < 16; ++kk) { QK1(aq, 0, kk, 0); QK1(aq, 0, kk, 1); RL(kk); }
#define KA(kk) QK1(ak, 1, kk, 0)
#define KB(kk) QK1(ak, 1, kk, 1)
    KA(0); KB(0); KA(1); KB(1); KA(2); KB(2); KA(3); KB(3);
    u32x4 qf = {pk(aq[0][0], aq[0][1]), pk(aq[0][2], aq[0][3]), pk(aq[1][0], aq[1][1]), pk(aq[1][2], aq[1][3])}; SB();
    KA(4); KB(4);
    f32x4 s0, s1, s2;
    MV0(s0, kx[0], qf); SB();
    MV0(s1, kx[1], qf); SB();
    KA(5); KB(5); KA(6); KB(6); KA(7); KB(7); KA(8); KB(8);
    scw[0] = s0; SB(); scw[32] = s1; SB();
    KA(9); KB(9); KA(10);
    LBAR();
    f32x4 xa = scx[0], xb = scx[96], xc = scx[192], xd = scx[288];
    SB();
    KB(10); KA(11); KB(11); KA(12); KB(12);
    unsigned nfl = LD32(flOff, oFH + ((s + 1) & 63) * 8192); SB();
    KA(13);
    xa = xa + xb; SB();
    KB(13);
    xc = xc + xd; SB();
    KA(14);
    xa = xa + xc; SB();
    KB(14);
    float mx, tm;
    asm volatile("v_max3_f32 %0, %1, %2, %3\n\tv_max_f32 %0, %0, %4" : "=&v"(mx) : "v"(xa[0]), "v"(xa[1]), "v"(xa[2]), "v"(xa[3])); SB();
    KA(15); KB(15);
    asm volatile("v_max_f32_dpp %0, %0, %0 row_ror:8 row_mask:0xf bank_mask:0xf\n\tv_mov_b32 %1, %0" : "+v"(mx), "=&v"(tm)); SB();
    V1(0, 0); V1(0, 1);
    asm volatile("v_permlane32_swap_b32 %0, %1\n\tv_max_f32 %0, %0, %1\n\tv_mov_b32 %1, %0" : "+v"(mx), "+v"(tm)); SB();
    V1(1, 0); V1(1, 1);
    asm volatile("v_permlane16_swap_b32 %0, %1\n\tv_max_f32 %0, %0, %1" : "+v"(mx), "+v"(tm)); SB();
    float nm = -mx * cexp; PINV(nm); SB();
    V1(2, 0);
    u32x4 kf = {pk(ak[0][0], ak[0][1]), pk(ak[0][2], ak[0][3]), pk(ak[1][0], ak[1][1]), pk(ak[1][2], ak[1][3])}; SB();
    V1(2, 1); V1(3, 0);
    MV0(s2, kf, qf); SB();
    float e0 = __builtin_amdgcn_exp2f(fmaf(xa[0], cexp, nm)), e1 = __builtin_amdgcn_exp2f(fmaf(xa[1], cexp, nm)); PINV(e0); PINV(e1); SB();
    V1(3, 1);
    float e2 = __builtin_amdgcn_exp2f(fmaf(xa[2], cexp, nm)), e3 = __builtin_amdgcn_exp2f(fmaf(xa[3], cexp, nm)); PINV(e2); PINV(e3); SB();
    V1(4, 0);
    scw[64] = s2; SB();
    unsigned p0 = pk(e0, e1), p1 = pk(e2, e3); PINV(p0); PINV(p1); SB();
    V1(4, 1);
    u32x4 pf = {p0, p1, DPP8(p0), DPP8(p1)}; PINV(pf); SB();
    V1(5, 0);
    u32x4 ones;
    asm volatile("v_mov_b32 %0, 0x3f803f80\n\tv_mov_b32 %1, 0x3f803f80\n\tv_mov_b32 %2, 0x3f803f80\n\tv_mov_b32 %3, 0x3f803f80" : "=v"(ones.x), "=v"(ones.y), "=v"(ones.z), "=v"(ones.w)); PINV(ones); SB();
    V1(5, 1);
    LBAR();
    f32x2 ma = scm[0], mb = scm[192], mc = scm[384], md = scm[576];
    wf[2][0][14] = wx[0]; wf[2][1][14] = wx[128]; wf[2][0][15] = wx[64];
    SB();
    f32x4 ao[2], al;
    MV0(ao[0], vx[0], pf); SB();
    MV0(ao[1], vx[1], pf); SB();
    MV0(al, ones, pf); SB();
    V1(6, 0);
    ST128(kf, kmOff, oKM + s * 65536); SB();
    V1(6, 1); V1(7, 0);
    ma = ma + mb; mc = mc + md; SB();
    V1(7, 1);
    ma = ma + mc;
    if (g >= 2) { ma[0] = -INFINITY; ma[1] = -INFINITY; }
    SB();
    V1(8, 0);
    float mm;
    asm volatile("v_max3_f32 %0, %1, %2, %3" : "=&v"(mm) : "v"(ma[0]), "v"(ma[1]), "v"(mx)); SB();
    V1(8, 1); V1(9, 0);
    asm volatile("v_max_f32_dpp %0, %0, %0 row_ror:8 row_mask:0xf bank_mask:0xf\n\tv_mov_b32 %1, %0" : "+v"(mm), "=&v"(tm)); SB();
    V1(9, 1); V1(10, 0);
    asm volatile("v_permlane32_swap_b32 %0, %1\n\tv_max_f32 %0, %0, %1\n\tv_mov_b32 %1, %0" : "+v"(mm), "+v"(tm)); SB();
    V1(10, 1); V1(11, 0);
    asm volatile("v_permlane16_swap_b32 %0, %1\n\tv_max_f32 %0, %0, %1" : "+v"(mm), "+v"(tm)); SB();
    float nm2 = -mm * cexp; PINV(nm2); SB();
    V1(11, 1);
    float e4 = __builtin_amdgcn_exp2f(fmaf(ma[0], cexp, nm2)), e5 = __builtin_amdgcn_exp2f(fmaf(ma[1], cexp, nm2)); PINV(e4); PINV(e5); SB();
    V1(12, 0);
    float fx = __builtin_amdgcn_exp2f(fmaf(mx, cexp, nm2));
    unsigned p2 = pk(e4, e5); PINV(fx); PINV(p2); SB();
    V1(12, 1);
    u32x4 pm4 = {p2, DPP8(p2), 0u, 0u}; PINV(pm4); SB();
    V1(13, 0);
    al[0] *= fx; PINV(al); SB();
    V1(13, 1);
    ao[0] *= fx; PINV(ao[0]); SB();
    V1(14, 0);
    MV1(al, ones, pm4); SB();
    ao[1] *= fx; PINV(ao[1]); SB();
    V1(14, 1); V1(15, 0); V1(15, 1);
    NOP(5); SB();
    u32x4 vm0 = {pk(av[0][0], av[0][1]), pk(av[0][2], av[0][3]), 0u, 0u}; SB();
    u32x4 vm1 = {pk(av[1][0], av[1][1]), pk(av[1][2], av[1][3]), 0u, 0u}; SB();
    MV1(ao[0], vm0, pm4); SB();
    float inv = __builtin_amdgcn_rcpf(al[0]); PINV(inv); SB();
    MV1(ao[1], vm1, pm4); SB();
    #pragma unroll
    for (int k = 16 - WOPRE; k < 16; ++k) wo[k] = wop[k * 64];
    NOP(7 - WOPRE); SB();
    u32x4 ofr = {pk(ao[0][0] * inv, ao[0][1] * inv), pk(ao[0][2] * inv, ao[0][3] * inv), pk(ao[1][0] * inv, ao[1][1] * inv), pk(ao[1][2] * inv, ao[1][3] * inv)};
    *ofw = ofr;
    SB();
    { u32x2 t0 = {vm0.x, vm0.y}, t1 = {vm1.x, vm1.y};
      ST64(t0, vmOff, oVMT + s * 65536);
      ST64(t1, vmOff + 256, oVMT + s * 65536); }
    if (s == 63) break;
    SB();
    LBAR();
    bf16x8 ob[4];
    #pragma unroll
    for (int ks = 0; ks < 4; ++ks) ob[ks] = ofr_p[ks * 32];
    SB();
    const unsigned tag = (s / PCRING) & 1, tag0 = tag | (tstamp << 1);
    const unsigned oPC = oPC0 + (s % PCRING) * 1048576;
    f32x4 accA[2], accB[2];
    #pragma unroll
    for (int pr = 0; pr < 5; ++pr) {
      u32x4 o;
      #pragma unroll
      for (int ks = 0; ks < 4; ++ks) {
        if (pr < 4) { if (ks == 0) MV0(accA[pr & 1], wo[(pr & 1) * 8 + ks], ob[ks]); else MV1(accA[pr & 1], wo[(pr & 1) * 8 + ks], ob[ks]); SB(); }
        if (pr < 4) { if (ks == 0) MV0(accB[pr & 1], wo[(pr & 1) * 8 + 4 + ks], ob[ks]); else MV1(accB[pr & 1], wo[(pr & 1) * 8 + 4 + ks], ob[ks]); SB(); }
        if (pr == 4 && ks == 0) { NOP(7); SB(); }
        if (pr > 0 && (ks > 1 || pr == 4)) {
          #pragma unroll
          for (int e = (pr == 4 ? ks : (ks - 2) * 2); e < (pr == 4 ? ks + 1 : (ks - 2) * 2 + 2); ++e) { float v = hi ? accB[(pr - 1) & 1][e] : accA[(pr - 1) & 1][e]; o[e] = e ? ((__float_as_uint(v) & ~1u) | tag) : ((__float_as_uint(v) & ~0x1ffu) | tag0); }
          SB(); }
        if (pr < 2) { wo[(pr & 1) * 8 + ks] = wop[((pr + 2) * 8 + ks) * 64]; wo[(pr & 1) * 8 + 4 + ks] = wop[((pr + 2) * 8 + 4 + ks) * 64]; SB(); }
      }
      if (pr > 0) { ST128(o, pcwOff, oPC + ((w * 8 + 2 * (pr - 1)) * 32 + h) * 512); SB(); }
    }
    const bool kv_ready = __all(nfl != EMPTY);
    {
      const unsigned ko = kv_ready ? (unsigned)kOff : OOB, vo = kv_ready ? (unsigned)vOff : OOB;
      #pragma unroll
      for (int t = 0; t < 2; ++t) { kx[t] = LD128(ko, oK + (s + 1) * 262144 + t * 4096); vx[t] = LD128(vo, oVT + (s + 1) * 262144 + t * 1024); }
      asm volatile("s_waitcnt vmcnt(8)" ::: "memory");
    }
    signal_cu(ws, cnt, oFH + s * 8192 + 128, lane);
    {
      SLEEPN(DELAY_A, (p.role_mask >> 8) & 255);
      u32x4 x[4];
      unsigned spins = 0;
      for (; spins < SPIN_CAP; ++spins) {
        MEMFENCE();
        #pragma unroll
        for (int k = 0; k < 4; ++k) x[k] = LD128(pcrOff + k * 512, oPC + h * 16384);
        unsigned an = 1, orr = 0;
        #pragma unroll
        for (int k = 0; k < 4; ++k) { an &= x[k].x & x[k].y & x[k].z & x[k].w; orr |= x[k].x | x[k].y | x[k].z | x[k].w; }
        bool ok = tag ? (an & 1) : !(orr & 1);
        if (__all(ok)) break;
      }
      if (spins >= SPIN_CAP && lane == 0) ST32(1u, 0u, O_ABT);
      DRAIN();
      u32x4 y[4];
      #pragma unroll
      for (int k = 0; k < 4; ++k) { y[k] = x[k]; y[k].x &= ~0x1ffu; }
      f32x4 a = (__builtin_bit_cast(f32x4, y[0]) + __builtin_bit_cast(f32x4, y[1])) + (__builtin_bit_cast(f32x4, y[2]) + __builtin_bit_cast(f32x4, y[3]));
      #pragma unroll
      for (int e = 0; e < 4; ++e) {
        float v = a[e];
        v += __uint_as_float(DPP8(__float_as_uint(v)));
        a[e] = red_sum(v);
      }
      u32x2 o = {pk(a[0], a[1]), pk(a[2], a[3])};
      if (o.x == EMPTY) o.x = 0xfffefffeu;
      if (o.y == EMPTY) o.y = 0xfffefffeu;
      ST64(o, memOff, oMEM + (s + 1) * 8192 + (h >> 1) * 512 + (h & 1) * 8);
      {
        const unsigned now = ((unsigned)__builtin_amdgcn_s_memrealtime() >> 1);
        unsigned age = min(min((now - (x[0].x >> 1)) & 255u, (now - (x[1].x >> 1)) & 255u), min((now - (x[2].x >> 1)) & 255u, (now - (x[3].x >> 1)) & 255u));
        age = min(age, DPP8(age));
        { auto r = __builtin_amdgcn_permlane32_swap(age, age, false, false); age = min(r[0], r[1]); }
        { auto r = __builtin_amdgcn_permlane16_swap(age, age, false, false); age = min(r[0], r[1]); }
        const int rem = (int)KB_VAL - (int)__builtin_amdgcn_readfirstlane(age);
        nsleep = rem > 0 ? (min(rem, 36) * 3) >> 2 : 0;
      }
      if (!kv_ready) {
        (void)wait_flags(ws, oFH + (s + 1) * 8192 + 4, 2, 4, lane);
        #pragma unroll
        for (int t = 0; t < 2; ++t) { kx[t] = LD128(kOff, oK + (s + 1) * 262144 + t * 4096); vx[t] = LD128(vOff + t * 1024, oVT + (s + 1) * 262144); }
      }
    }
  }
  DRAIN();
  signal_cu(ws, cnt, oFH + 63 * 8192 + 128, lane);
#undef V1
#undef RL
#undef QK1
#undef K2
#undef KA
#undef KB
}

template <bool PERM>
__device__ __forceinline__ void stage_x(char* lx, const u32x4 (&xr)[8], int tid) {
  char* b = lx + (tid & 63) * 528 + (tid >> 6) * 16;
  #pragma unroll
  for (int k = 0; k < 8; ++k) *(u32x4*)(b + (PERM ? (k & 1) * 256 + (k >> 1) * 64 : k * 64)) = xr[k];
}
#define XFRAG(base, kk, rt, tokS) (*(const bf16x8*)((base) + (kk) * 2112 + (rt) * (tokS) * 16))

template <bool PERM>
__device__ __forceinline__ void store_partial(rsrc_t ws, char* tp, const f32x4 (&acc)[8][2], unsigned obase, int w, int lane) {
  const int i = lane & 15, g = lane >> 4;
  char* wp = tp + (PERM ? 8 * (i >> 2) + (i & 3) : i) * 272 + g * 16;
  const char* rp = tp + g * 272 + i * 16;
  const unsigned so = g * 1024 + w * 256 + i * 16;
  #pragma unroll
  for (int rt = 0; rt < 2; ++rt)
  #pragma unroll
  for (int c = 0; c < 4; ++c) {
    u32x4 o = {pk(acc[2 * c][rt][0], acc[2 * c][rt][1]), pk(acc[2 * c][rt][2], acc[2 * c][rt][3]), pk(acc[2 * c + 1][rt][0], acc[2 * c + 1][rt][1]), pk(acc[2 * c + 1][rt][2], acc[2 * c + 1][rt][3])};
    *(u32x4*)(wp + rt * (PERM ? 1088 : 4352) + c * 64) = o;
  }
  u32x4 v[8];
  #pragma unroll
  for (int k = 0; k < 8; ++k) v[k] = *(const u32x4*)(rp + k * 1088);
  #pragma unroll
  for (int k = 0; k < 8; ++k) ST128(v[k], so, obase + k * 4096);
}

template <int MM>
__device__ __forceinline__ void role_qkv(const Params& p, rsrc_t ws, char* lds, const int m, const int h) {
  const int tid = threadIdx.x, lane = tid & 63, w = __builtin_amdgcn_readfirstlane(tid >> 6), i = lane & 15, g = lane >> 4;
  int* dec = (int*)(lds + 2 * 34816);
  bf16x8 wf[2][16], wq[16];
  {
    const bf16x8* src = (const bf16x8*)(p.wsp + O_WQKV) + (size_t)(((h * 4 + w) * 3 + m) * 32) * 64 + lane;
    #pragma unroll
    for (int t = 0; t < 2; ++t)
    #pragma unroll
    for (int kk = 0; kk < 16; ++kk) wf[t][kk] = src[(t * 16 + kk) * 64];
    const bf16x8* sq = (const bf16x8*)(p.wsp + O_WQKV) + (size_t)(((h * 4 + w) * 3) * 32 + (MM - 1) * 16) * 64 + lane;
    #pragma unroll
    for (int kk = 0; kk < 16; ++kk) wq[kk] = sq[kk * 64];
    SB();
    #pragma unroll
    for (int kk = 0; kk < 16; ++kk) { PINA(wq[kk]); PINA(wf[0][kk]); }
    SB();
  }
  const int prm = 8 * (i >> 2) + (i & 3);
  constexpr bool ISV = MM == 2;
  constexpr int tokS = 16;
  const char* xb = lds + g * 528 + i * 16;
  const unsigned stOff = ISV ? (unsigned)(w * 2048 + lane * 16) : (unsigned)(w * 1024 + lane * 16);
  int n0 = (p.layer_mask & 1) ? 0 : 64, n1 = (p.layer_mask & 2) ? 0 : 64;
  const unsigned flv = lane < 32 ? (unsigned)lane * 128u : OOB;
  u32x4 xr[8];
  int cur = -1;
  const bool throttle = (p.role_mask & 1) != 0;
  const unsigned l0 = lane == 0 ? 0u : OOB;
  bool credit = false;
  auto poll = [&](unsigned& f1, unsigned& f0, unsigned& fk) {
    f1 = n1 < 64 ? LD32(flv, O_FXN + (64 + n1) * 4096) : EMPTY;
    f0 = n0 < 64 ? 0u : EMPTY;
    fk = (throttle && !credit && n0 >= QKV_AHEAD && n0 < 64) ? LD32(l0, O_FH + ((n0 - QKV_URGENT) * 32 + h) * 256 + 128) : 0u;
  };
  auto decide = [&](unsigned f1, unsigned f0, unsigned fk) -> int { return __all(f1 != EMPTY) ? 64 + n1 : (__all(f0 != EMPTY && fk != EMPTY) ? n0 : -1); };
  auto pick_blocking = [&]() -> int {
    if (w == 0) {
      int d = -2;
      for (unsigned spins = 0; spins < SPIN_CAP; ++spins) {
        MEMFENCE();
        unsigned f1, f0, fk; poll(f1, f0, fk);
        const int dd = decide(f1, f0, fk);
        if (dd >= 0) { d = dd; break; }
        const unsigned long long m1 = __ballot(f1 == EMPTY);
        const unsigned a1 = (n1 < 64 && lane == (int)__builtin_ctzll(m1)) ? flv : OOB;
        const bool w0 = n0 < 64 && __all(f0 != EMPTY);
        bool ab = false;
        for (; spins < SPIN_CAP; ++spins) {
          __builtin_amdgcn_s_sleep(POLL_SLEEP);
          MEMFENCE();
          if (n0 < 64 && !w0) break;
          const unsigned g1 = n1 < 64 ? LD32(a1, O_FXN + (64 + n1) * 4096) : EMPTY;
          const unsigned gk = w0 ? LD32(l0, O_FH + ((n0 - QKV_URGENT) * 32 + h) * 256 + 128) : EMPTY;
          if (__all(g1 != EMPTY) || __all(gk != EMPTY)) break;
          if ((spins & 63u) == 63u) { if (__builtin_amdgcn_readfirstlane(LD32(0u, O_ABT)) == 1u) { ab = true; break; } }
        }
        if (ab) break;
      }
      if (d == -2 && lane == 0) ST32(1u, 0u, O_ABT);
      if (lane == 0) dec[1] = d;
    }
    LBAR();
    return __builtin_amdgcn_readfirstlane(dec[1]);
  };
  auto issue = [&](int item) {
    #pragma unroll
    for (int k = 0; k < 8; ++k) xr[k] = LD128(tid * 16, O_XN + item * 32768 + k * 4096);
    if (item >= 64) { ++n1; credit = true; } else { ++n0; credit = false; }
  };
  if (n0 >= 64 && n1 >= 64) return;
  unsigned* cnt = (unsigned*)(lds + 2 * 34816 + 64);
  if (tid == 0) *cnt = 0u;
  cur = pick_blocking();
  if (cur < 0) return;
  issue(cur);
  const f32x4 z = {0, 0, 0, 0};
  int buf = 0; unsigned pend = 0;
  for (;;) {
    unsigned f1 = EMPTY, f0 = EMPTY, fk = EMPTY;
    stage_x<true>(lds + buf * 34816, xr, tid);
    if (pend) { DRAIN(); signal_cu(ws, cnt, pend, lane); }
    if (w == 0) poll(f1, f0, fk);
    LBAR();
    const char* xq = xb + buf * 34816;
    bf16x8 xf[4][2];
    #pragma unroll
    for (int kk = 0; kk < 4; ++kk) { xf[kk][0] = XFRAG(xq, kk, 0, tokS); xf[kk][1] = XFRAG(xq, kk, 1, tokS); }
    SB();
    f32x4 acc[2][2], aq[2];
    int nxt = -1, nxtv = -1; unsigned nso = 0, nvo = OOB;
    #pragma unroll
    for (int kk = 0; kk < 16; ++kk) {
      #pragma unroll
      for (int t = 0; t < 2; ++t)
      #pragma unroll
      for (int rt = 0; rt < 2; ++rt) {
        if (ISV) acc[t][rt] = MFMA(xf[kk & 3][rt], wf[t][kk], kk ? acc[t][rt] : z);
        else acc[t][rt] = MFMA(wf[t][kk], xf[kk & 3][rt], kk ? acc[t][rt] : z);
      }
      aq[0] = MFMA(wq[kk], xf[kk & 3][0], kk ? aq[0] : z);
      aq[1] = MFMA(wq[kk], xf[kk & 3][1], kk ? aq[1] : z);
      SB();
      if (kk + 4 < 16) { xf[kk & 3][0] = XFRAG(xq, kk + 4, 0, tokS); xf[kk & 3][1] = XFRAG(xq, kk + 4, 1, tokS); }
      if (kk == 6) {
        if (w == 0) { int d = decide(f1, f0, fk); if (lane == 0) *dec = d; }
        LBAR();
        nxtv = *dec;
      }
      if (kk == 7) { nxt = __builtin_amdgcn_readfirstlane(nxtv); nso = O_XN + (nxt & 127) * 32768; nvo = nxt >= 0 ? (unsigned)tid * 16u : OOB; }
      if (kk >= 8) xr[kk - 8] = LD128(nvo, nso + (kk - 8) * 4096);
      SB();
    }
    if (nxt >= 64) { ++n1; credit = true; } else if (nxt >= 0) { ++n0; credit = false; }
    const int s = cur & 63, l = cur >> 6;
    const unsigned ob = O_Q + m * QKV_SZ + (s * 32 + h) * 8192;
    if (ISV) {
      #pragma unroll
      for (int t = 0; t < 2; ++t) {
        u32x4 o = {pk(acc[t][0][0], acc[t][0][1]), pk(acc[t][0][2], acc[t][0][3]), pk(acc[t][1][0], acc[t][1][1]), pk(acc[t][1][2], acc[t][1][3])};
        ST128(o, stOff + t * 1024, ob);
      }
    } else {
      #pragma unroll
      for (int rt = 0; rt < 2; ++rt) {
        u32x4 o = {pk(acc[0][rt][0], acc[0][rt][1]), pk(acc[0][rt][2], acc[0][rt][3]), pk(acc[1][rt][0], acc[1][rt][1]), pk(acc[1][rt][2], acc[1][rt][3])};
        ST128(o, stOff, ob + rt * 4096);
      }
    }
    #pragma unroll
    for (int rt = 0; rt < 2; ++rt) {
      u32x2 o = {pk(aq[rt][0], aq[rt][1]), pk(aq[rt][2], aq[rt][3])};
      ST64(o, w * 1024 + lane * 16 + (MM - 1) * 8, O_Q + (s * 32 + h) * 8192 + rt * 4096);
    }
    pend = O_FH + ((l * 64 + s) * 32 + h) * 256 + m * 4;
    if (nxt < 0) {
      DRAIN();
      signal_cu(ws, cnt, pend, lane); pend = 0;
      if (n0 >= 64 && n1 >= 64) break;
      nxt = pick_blocking();
      if (nxt < 0) break;
      issue(nxt);
    }
    cur = nxt; buf ^= 1;
  }
}

__device__ __forceinline__ void role_att(const Params& p, rsrc_t ws, char* lds, const int l, const int a, const int par) {
  const int tid = threadIdx.x, lane = tid & 63, w = __builtin_amdgcn_readfirstlane(tid >> 6), i = lane & 15, g = lane >> 4;
  u32x4* ofx = (u32x4*)lds + lane;
  bf16x8* lwo = (bf16x8*)(lds + 32768 + 4 * 8704) + w * 1024 + lane;
  bf16x8 wo[2][8][4];
  #pragma unroll
  for (int hh = 0; hh < 2; ++hh) {
    const bf16x8* src = (const bf16x8*)(p.wsp + O_WO) + (size_t)((2 * a + hh) * 32 + w * 8) * 256 + lane;
    #pragma unroll
    for (int c = 0; c < 8; ++c)
    #pragma unroll
    for (int ks = 0; ks < 4; ++ks) { wo[hh][c][ks] = src[(c * 4 + ks) * 64]; if (hh == 1 && c >= 4) lwo[((c - 4) * 4 + ks) * 64] = wo[hh][c][ks]; }
    SB();
    #pragma unroll
    for (int c = 0; c < 8; ++c)
    #pragma unroll
    for (int ks = 0; ks < 4; ++ks) { if (hh * 32 + c * 4 + ks < 44) PINA(wo[hh][c][ks]); }
    SB();
  }
  const int hh = w >> 1, qh = w & 1, h = 2 * a + hh;
  const int prm = 8 * (i >> 2) + (i & 3);
  const int tq = prm + 4 * qh;
  const float cexp = 0.08838834764831845f * 1.4426950408889634f;
  const f32x4 z = {0, 0, 0, 0};
  const unsigned l16 = lane * 16, kmOff = (g * 8 + (i & 7)) * 16, vmOff = (lane & 31) * 8;
  const unsigned oFH = O_FH + (l * 64 * 32 + h) * 256;
  unsigned* cnt = (unsigned*)(lds + 32768 + 4 * 8704 + 65536);
  if (tid == 0) *cnt = 0u;
  u32x4 qf[4], kf[2][4], vf[8];
  auto issue = [&](int s) {
    const unsigned oq = O_Q + (s * 32 + h) * 8192;
    #pragma unroll
    for (int ks = 0; ks < 4; ++ks) {
      qf[ks] = LD128(l16 + ks * 1024, oq + qh * 4096);
      kf[0][ks] = LD128(l16 + ks * 1024, oq + QKV_SZ);
      kf[1][ks] = LD128(l16 + ks * 1024, oq + QKV_SZ + 4096);
    }
    #pragma unroll
    for (int t8 = 0; t8 < 8; ++t8) vf[t8] = LD128(l16 + (t8 & 3) * 1024, oq + 2 * QKV_SZ + (t8 >> 2) * 4096);
    SB();
  };
  u32x4 kmf[4]; u32x2 vmf[8];
  auto issue_km = [&](int s) {
    const unsigned okm = ((l * 64 + s) * 32 + h) * 2048;
    #pragma unroll
    for (int ks = 0; ks < 4; ++ks) kmf[ks] = LD128(kmOff + ks * 512, O_KM + okm);
    #pragma unroll
    for (int t8 = 0; t8 < 8; ++t8) vmf[t8] = LD64(vmOff + t8 * 256, O_VMT + okm);
    SB();
  };
  const unsigned flv = lane < 2 ? (unsigned)lane * 4u + 4u : (lane == 3 ? 128u : OOB);
  if (!wait_flags(ws, oFH + par * 8192 + 4, 2, 4, lane)) return;
  issue(par);
  bool early = false;
  for (int s = par; s < 64; s += 2) {
    f32x4 s0, s1, s2;
    if (!early) {
      #pragma unroll
      for (int ks = 0; ks < 4; ++ks) {
        s0 = MFMA(BC8(kf[0][ks]), BC8(qf[ks]), ks ? s0 : z);
        s1 = MFMA(BC8(kf[1][ks]), BC8(qf[ks]), ks ? s1 : z);
      }
      SB();
      if (!wait_flags(ws, oFH + s * 8192 + 128, 1, 4, lane)) return;
      issue_km(s);
    } else {
      #pragma unroll
      for (int ks = 0; ks < 4; ++ks) {
        s0 = MFMA(BC8(kf[0][ks]), BC8(qf[ks]), ks ? s0 : z);
        s1 = MFMA(BC8(kf[1][ks]), BC8(qf[ks]), ks ? s1 : z);
      }
      SB();
    }
    float sv[12];
    #pragma unroll
    for (int b = 0; b < 4; ++b) {
      sv[b] = (8 * g + b <= tq) ? s0[b] : -INFINITY;
      sv[4 + b] = (8 * g + 4 + b <= tq) ? s1[b] : -INFINITY;
    }
    float mx = fmaxf(fmaxf(fmaxf(sv[0], sv[1]), fmaxf(sv[2], sv[3])), fmaxf(fmaxf(sv[4], sv[5]), fmaxf(sv[6], sv[7])));
    SB();
    #pragma unroll
    for (int ks = 0; ks < 4; ++ks) s2 = MFMA(BC8(kmf[ks]), BC8(qf[ks]), ks ? s2 : z);
    #pragma unroll
    for (int b = 0; b < 4; ++b) { sv[8 + b] = g < 2 ? s2[b] : -INFINITY; mx = fmaxf(mx, sv[8 + b]); }
    mx = red_max(mx);
    const float nm = -mx * cexp;
    float sum = 0;
    #pragma unroll
    for (int b = 0; b < 12; ++b) { sv[b] = __builtin_amdgcn_exp2f(fmaf(sv[b], cexp, nm)); sum += sv[b]; }
    sum = red_sum(sum);
    const float inv = __builtin_amdgcn_rcpf(sum);
    u32x4 pf = {pk(sv[0] * inv, sv[1] * inv), pk(sv[2] * inv, sv[3] * inv), pk(sv[4] * inv, sv[5] * inv), pk(sv[6] * inv, sv[7] * inv)};
    u32x4 pm = {pk(sv[8] * inv, sv[9] * inv), pk(sv[10] * inv, sv[11] * inv), 0u, 0u};
    f32x4 oo[8];
    #pragma unroll
    for (int t8 = 0; t8 < 8; ++t8) oo[t8] = MFMA(BC8(vf[t8]), BC8(pf), z);
    #pragma unroll
    for (int t8 = 0; t8 < 8; ++t8) { u32x4 v0 = {vmf[t8].x, vmf[t8].y, 0u, 0u}; oo[t8] = MFMA(BC8(v0), BC8(pm), oo[t8]); }
    u32x4* ofw = ofx + ((s >> 1) & 1) * 1024;
    #pragma unroll
    for (int ks = 0; ks < 4; ++ks) {
      u32x4 o = {pk(oo[2 * ks][0], oo[2 * ks][1]), pk(oo[2 * ks][2], oo[2 * ks][3]), pk(oo[2 * ks + 1][0], oo[2 * ks + 1][1]), pk(oo[2 * ks + 1][2], oo[2 * ks + 1][3])};
      ofw[(w * 4 + ks) * 64] = o;
    }
    SB();
    if (early) { DRAIN(); signal_cu(ws, cnt, O_FPA + ((l * 64 + s - 2) * 16 + a) * 128, lane); }
    unsigned nfl = LD32(flv, oFH + ((s + 2) & 63) * 8192);
    LBAR();
    f32x4 acc[8][2];
    bf16x8 b0 = BC8(ofw[0]), b1 = BC8(ofw[256]);
    bool pre = false;
    unsigned pvo = OOB, kvo = OOB, vvo = OOB;
    const unsigned noq = O_Q + (((s + 2) & 63) * 32 + h) * 8192, nokm = ((l * 64 + ((s + 2) & 63)) * 32 + h) * 2048;
    #pragma unroll
    for (int q = 0; q < 8; ++q) {
      const int h2 = q >> 2, ks = q & 3;
      bf16x8 n0, n1;
      if (q < 7) { n0 = BC8(ofw[((((q + 1) >> 2) * 2 + 0) * 4 + ((q + 1) & 3)) * 64]); n1 = BC8(ofw[((((q + 1) >> 2) * 2 + 1) * 4 + ((q + 1) & 3)) * 64]); }
      if (q == 2) {
        pre = s < 62 && __all(lane >= 3 || nfl != EMPTY); early = pre && __all(nfl != EMPTY);
        pvo = pre ? l16 : OOB; kvo = early ? kmOff : OOB; vvo = early ? vmOff : OOB;
      }
      if (q >= 4) {
        #pragma unroll
        for (int c = 4; c < 8; ++c) wo[1][c][ks] = lwo[((c - 4) * 4 + ks) * 64];
      }
      SB();
      #pragma unroll
      for (int c = 0; c < 8; ++c) {
        acc[c][0] = MFMA(wo[h2][c][ks], b0, q ? acc[c][0] : z);
        acc[c][1] = MFMA(wo[h2][c][ks], b1, q ? acc[c][1] : z);
        const int j = (q - 2) * 8 + c;
        if (q >= 2 && j < 32) {
          SB();
          if (j < 4) qf[j] = LD128(pvo + j * 1024, noq + qh * 4096);
          else if (j < 12) kf[(j - 4) >> 2][j & 3] = LD128(pvo + (j & 3) * 1024, noq + QKV_SZ + ((j - 4) >> 2) * 4096);
          else if (j < 20) vf[j - 12] = LD128(pvo + (j & 3) * 1024, noq + 2 * QKV_SZ + ((j - 12) >> 2) * 4096);
          else if (j < 24) kmf[j - 20] = LD128(kvo + (j - 20) * 512, O_KM + nokm);
          else vmf[j - 24] = LD64(vvo + (j - 24) * 256, O_VMT + nokm);
          SB();
        }
      }
      SB();
      if (q < 7) { b0 = n0; b1 = n1; }
    }
    store_partial<true>(ws, lds + 32768 + w * 8704, acc, O_PA + (PSLOT(l, s) * 16 + a) * 32768, w, lane);
    if (!early) {
      DRAIN();
      signal_cu(ws, cnt, O_FPA + ((l * 64 + s) * 16 + a) * 128, lane);
      if (s < 62 && !pre) { if (!wait_flags(ws, oFH + (s + 2) * 8192 + 4, 2, 4, lane)) return; issue(s + 2); }
    }
  }
}

__device__ __forceinline__ void role_ffn(const Params& p, rsrc_t ws, char* lds, const int l, const int f) {
  const int tid = threadIdx.x, lane = tid & 63, w = __builtin_amdgcn_readfirstlane(tid >> 6), i = lane & 15, g = lane >> 4;
  u32x4* hx = (u32x4*)(lds + 34816) + lane;
  bf16x8* lwd = (bf16x8*)(lds + 34816 + 8192) + w * 1024 + lane;
  bf16x8 wg[2][2][16], wd[4][4];
  {
    const bf16x8* src = (const bf16x8*)(p.wsp + O_WGU) + (size_t)((f * 4 + w) * 64) * 64 + lane;
    #pragma unroll
    for (int m = 0; m < 2; ++m)
    #pragma unroll
    for (int t = 0; t < 2; ++t) {
      #pragma unroll
      for (int kk = 0; kk < 16; ++kk) wg[m][t][kk] = src[((m * 2 + t) * 16 + kk) * 64];
      SB();
      #pragma unroll
      for (int kk = 0; kk < 16; ++kk) { if ((m * 2 + t) * 16 + kk < 44) PINA(wg[m][t][kk]); }
      SB();
    }
    const bf16x8* sd = (const bf16x8*)(p.wsp + O_WD) + (size_t)(f * 32 + w * 8) * 256 + lane;
    #pragma unroll
    for (int c = 0; c < 4; ++c)
    #pragma unroll
    for (int ks = 0; ks < 4; ++ks) { wd[c][ks] = sd[(c * 4 + ks) * 64]; lwd[(c * 4 + ks) * 64] = sd[((c + 4) * 4 + ks) * 64]; }
  }
  const f32x4 z = {0, 0, 0, 0};
  const char* xb = lds + g * 528 + i * 16;
  const unsigned flv = lane < 32 ? (unsigned)lane * 128u : OOB;
  unsigned* cnt = (unsigned*)(lds + 34816 + 8192 + 65536 + 4 * 8704);
  if (tid == 0) *cnt = 0u;
  u32x4 xr[8];
  auto issue = [&](int s) {
    #pragma unroll
    for (int k = 0; k < 8; ++k) xr[k] = LD128(tid * 16, O_XN1 + (l * 64 + s) * 32768 + k * 4096);
    SB();
  };
  bool early = false, seen = false;
  float hprev = 0.f;
  for (int s = 0; s < 64; ++s) {
    if (!early) { if (!seen && !wait_flags(ws, O_FXN1 + (l * 64 + s) * 4096, 32, 128, lane)) return; issue(s); }
    stage_x<false>(lds, xr, tid);
    LBAR();
    f32x4 ag[2][2][2];
    auto gelu1 = [&](int rt, int t, int b) -> float {
      const float gv = ag[0][t][rt][b], uv = ag[1][t][rt][b];
      const float y2 = gv * fmaf(gv * gv, -0.044715f * 2.302208198144325f, -2.302208198144325f);
      return gv * __builtin_amdgcn_rcpf(1.0f + __builtin_amdgcn_exp2f(y2)) * uv;
    };
    u32x4 ho;
    bf16x8 xf[4];
    #pragma unroll
    for (int kk = 0; kk < 4; ++kk) xf[kk] = XFRAG(xb, kk, 0, 16);
    SB();
    #pragma unroll
    for (int j = 0; j < 32; ++j) {
      const int rt = j >> 4, kk = j & 15;
      #pragma unroll
      for (int m = 0; m < 2; ++m) {
        #pragma unroll
        for (int t = 0; t < 2; ++t) ag[m][t][rt] = MFMA(wg[m][t][kk], xf[j & 3], kk ? ag[m][t][rt] : z);
        SB();
        if (rt == 1 && kk >= 2 && kk < 10 && m == 0) {
          const int e = kk - 2; float hv = gelu1(0, e >> 2, e & 3);
          if (e & 1) { ho[e >> 1] = pk(hprev, hv); } else hprev = hv;
          SB();
        }
      }
      if (j + 4 < 32) xf[j & 3] = XFRAG(xb, (j + 4) & 15, (j + 4) >> 4, 16);
      if (j == 6 && early) { DRAIN(); signal_cu(ws, cnt, O_FPF + ((l * 64 + s - 1) * 16 + f) * 128, lane); }
      if (j == 27) hx[w * 64] = ho;
      SB();
    }
    bf16x8 wl[4];
    #pragma unroll
    for (int c = 0; c < 4; ++c) wl[c] = lwd[(c * 4) * 64];
    unsigned nfl = LD32(flv, O_FXN1 + (l * 64 + ((s + 1) & 63)) * 4096);
    LBAR();
    f32x4 acc[8][2];
    unsigned nvo = OOB; const unsigned nso = O_XN1 + (l * 64 + ((s + 1) & 63)) * 32768;
    #pragma unroll
    for (int rt = 0; rt < 2; ++rt) {
      bf16x8 hb[4];
      #pragma unroll
      for (int ks = 0; ks < 4; ++ks) hb[ks] = BC8(hx[(rt * 4 + ks) * 64]);
      SB();
      #pragma unroll
      for (int ks = 0; ks < 4; ++ks) {
        bf16x8 wn[4];
        #pragma unroll
        for (int c = 0; c < 4; ++c) wn[c] = lwd[(c * 4 + ((ks + 1) & 3)) * 64];
        SB();
        #pragma unroll
        for (int c = 0; c < 8; ++c) {
          acc[c][rt] = MFMA(c < 4 ? wd[c & 3][ks] : wl[c & 3], hb[ks], ks ? acc[c][rt] : z);
          if (rt == 0 && (c & 3) == 1) {
            SB();
            const int e = ks * 2 + (c >> 2); float hv = gelu1(1, e >> 2, e & 3);
            if (e & 1) { ho[e >> 1] = pk(hprev, hv); } else hprev = hv;
            SB();
          }
          if (rt == 1 && (c & 3) == 1) { SB(); xr[ks * 2 + (c >> 2)] = LD128(nvo, nso + (ks * 2 + (c >> 2)) * 4096); SB(); }
        }
        SB();
        #pragma unroll
        for (int c = 0; c < 4; ++c) wl[c] = wn[c];
      }
      if (rt == 0) {
        hx[(4 + w) * 64] = ho;
        early = s < 63 && __all(nfl != EMPTY);
        nvo = early ? (unsigned)tid * 16u : OOB;
        LBAR();
      }
    }
    store_partial<false>(ws, lds + 34816 + 8192 + 65536 + w * 8704, acc, O_PF + (PSLOT(l, s) * 14 + f) * 32768, w, lane);
    if (!early) {
      const unsigned nf2 = LD32(flv, O_FXN1 + (l * 64 + ((s + 1) & 63)) * 4096);
      DRAIN();
      signal_cu(ws, cnt, O_FPF + ((l * 64 + s) * 16 + f) * 128, lane);
      seen = __all(nf2 != EMPTY);
    }
  }
}

__device__ __forceinline__ void role_red(const Params& p, rsrc_t ws, char* lds, const int r) {
  const int tid = threadIdx.x, lane = tid & 63, w = __builtin_amdgcn_readfirstlane(tid >> 6);
  u32x4* xres = (u32x4*)lds + lane;
  const int st = w & 1, l = w >> 1;
  if (!(p.layer_mask & (1 << l)) || !(p.role_mask & (st ? 16 : 8))) return;
  const int np = st ? 14 : 16;
  for (int s = 0; s < 64; ++s) {
    u32x4 xv;
    if (w == 0) xv = *(const u32x4*)(p.x + ((size_t)(s * 32 + r) * 512 + lane * 8));
    if (!wait_flags(ws, (st ? O_FPF : O_FPA) + (l * 64 + s) * 2048, np, 128, lane)) return;
    const unsigned ob = (st ? O_PF + PSLOT(l, s) * 14 * 32768 : O_PA + PSLOT(l, s) * 16 * 32768) + r * 1024;
    u32x4 a[16];
    #pragma unroll
    for (int k = 0; k < 16; ++k) { if (k < 14 || !st) a[k] = LD128(lane * 16, ob + k * 32768); else a[k] = (u32x4){0u, 0u, 0u, 0u}; }
    if (w > 0) xv = xres[s * 64];
    f32x4 sa = {0, 0, 0, 0}, sb = {0, 0, 0, 0};
    #pragma unroll
    for (int k = 0; k < 16; ++k) {
      sa += (f32x4){lo16(a[k].x), hi16(a[k].x), lo16(a[k].y), hi16(a[k].y)};
      sb += (f32x4){lo16(a[k].z), hi16(a[k].z), lo16(a[k].w), hi16(a[k].w)};
    }
    float v[8];
    v[0] = rb(lo16(xv.x) + rb(sa[0])); v[1] = rb(hi16(xv.x) + rb(sa[1])); v[2] = rb(lo16(xv.y) + rb(sa[2])); v[3] = rb(hi16(xv.y) + rb(sa[3]));
    v[4] = rb(lo16(xv.z) + rb(sb[0])); v[5] = rb(hi16(xv.z) + rb(sb[1])); v[6] = rb(lo16(xv.w) + rb(sb[2])); v[7] = rb(hi16(xv.w) + rb(sb[3]));
    u32x4 xo = {pk(v[0], v[1]), pk(v[2], v[3]), pk(v[4], v[5]), pk(v[6], v[7])};
    if (w == 3) {
      *(u32x4*)(p.out + ((size_t)(s * 32 + r) * 512 + lane * 8)) = xo;
    } else {
      xres[s * 64] = xo;
      const unsigned od = st ? O_XN + (64 + s) * 32768 : O_XN1 + (l * 64 + s) * 32768;
      ST128(ln_row(v), lane * 16, od + r * 1024);
      asm volatile("s_waitcnt vmcnt(0) lgkmcnt(0)" ::: "memory");
      if (lane == 0) ST32(1u, r * 128, st ? O_FXN + (64 + s) * 4096 : O_FXN1 + (l * 64 + s) * 4096);
    }
  }
}

__global__ void __launch_bounds__(256, 1) k_main(Params p) {
  extern __shared__ __attribute__((aligned(16))) char lds[];
  rsrc_t ws = __builtin_amdgcn_make_buffer_rsrc(p.wsp, 0, 256u * 1024 * 1024, 0x00020000);
#define MAPMODE 0
  int b = blockIdx.x;
  {
    const int x = b & 7, y = b >> 3;
    if (MAPMODE == 1) b = x < 2 ? x * 32 + y : 64 + y * 6 + (x - 2);
    if (MAPMODE == 2) b = y < 16 ? (x < 4 ? (x >> 1) * 32 + (x & 1) * 16 + y : 64 + y * 4 + (x - 4)) : 128 + (y - 16) * 8 + x;
    if (MAPMODE == 3) b = y < 8 ? (x >> 2) * 32 + (x & 3) * 8 + y : b;
  }
  if (b < 64) { if ((p.role_mask & 1) && (p.layer_mask & (1 << (b >> 5)))) role_chain(p, ws, lds, b >> 5, b & 31); }
  else if (b < 96) { if (p.role_mask & 2) role_qkv<2>(p, ws, lds, 2, b & 31); }
  else if (b < 128) { if (p.role_mask & 2) role_qkv<1>(p, ws, lds, 1, b & 31); }
  else if (b < 192) { const int l = (b - 128) >> 5; if ((p.role_mask & 4) && (p.layer_mask & (1 << l))) role_att(p, ws, lds, l, b & 15, (b >> 4) & 1); }
  else if (b < 220) { const int l = (b - 192) >= 14; if ((p.role_mask & 64) && (p.layer_mask & (1 << l))) role_ffn(p, ws, lds, l, b - 192 - 14 * l); }
  else if (b < 252) { if (p.role_mask & (8 | 16 | 32)) role_red(p, ws, lds, b - 220); }
}

extern "C" void kernel_launch(void* const* d_in, const int* in_sizes, int n_in, void* d_out, int out_size, void* d_ws, size_t ws_size, hipStream_t stream) {
  const u16* x = (const u16*)d_in[0]; const u16* mem0 = (const u16*)d_in[1];
  const u16* Wq = (const u16*)d_in[2]; const u16* Wk = (const u16*)d_in[3]; const u16* Wv = (const u16*)d_in[4]; const u16* Wo = (const u16*)d_in[5];
  const u16* wg = (const u16*)d_in[8]; const u16* wu = (const u16*)d_in[9]; const u16* wd = (const u16*)d_in[10];
  (void)hipFuncSetAttribute((const void*)k_main, hipFuncAttributeMaxDynamicSharedMemorySize, LDS_BYTES);
  k_prep<<<PREP_B + INIT_B, 256, 0, stream>>>(x, Wq, Wk, Wv, Wo, wg, wu, wd, (char*)d_ws);
  Params p{x, mem0, (u16*)d_out, (char*)d_ws, 127 | g_delay, 3};
  k_main<<<256, 256, LDS_BYTES, stream>>>(p);
}
```

```cpp
#include <hip/hip_runtime.h>
#include <hip/hip_bf16.h>
#include <stdint.h>

typedef unsigned short u16;
typedef short bf16x8 __attribute__((ext_vector_type(8)));
typedef float f32x4 __attribute__((ext_vector_type(4)));
typedef float f32x2 __attribute__((ext_vector_type(2)));
typedef unsigned u32x4 __attribute__((ext_vector_type(4)));
typedef unsigned u32x2 __attribute__((ext_vector_type(2)));
typedef __bf16 bfx2 __attribute__((ext_vector_type(2)));

constexpr unsigned O_FXN  = 0;
constexpr unsigned O_FXN1 = O_FXN + 524288;
constexpr unsigned O_FPA  = O_FXN1 + 524288;
constexpr unsigned O_FPF  = O_FPA + 262144;
constexpr unsigned O_ABT  = O_FPF + 262144;
constexpr unsigned O_FH   = O_ABT + 256;
constexpr unsigned O_MEM  = O_FH + 1048576;
constexpr unsigned O_PC   = O_MEM + 1048576;
#define PCRING 8
constexpr unsigned CTRL_BYTES = (O_PC + PCRING * 1048576 + 16383) & ~16383u;
constexpr unsigned O_WQKV = (CTRL_BYTES + 4095) & ~4095u;
constexpr unsigned O_WO   = O_WQKV + 3 * 512 * 4096 * 2;
constexpr unsigned O_WGU  = O_WO + 4096 * 512 * 2;
constexpr unsigned O_WD   = O_WGU + 2 * 512 * 1792 * 2;
constexpr unsigned O_XN   = O_WD + 1792 * 512 * 2;
constexpr unsigned O_XN1  = O_XN + 2 * 64 * 32768;
constexpr unsigned O_Q    = O_XN1 + 2 * 64 * 32768;
constexpr unsigned QKV_SZ = 64 * 32 * 8192;
constexpr unsigned O_KM   = O_Q + 3 * QKV_SZ;
constexpr unsigned O_VMT  = O_KM + 2 * 64 * 32 * 2048;
constexpr unsigned O_PA   = O_VMT + 2 * 64 * 32 * 2048;
constexpr unsigned O_PF   = O_PA + 64u * 16 * 32768;
constexpr unsigned WS_END = O_PF + 64u * 14 * 32768;
static_assert(WS_END <= 256u * 1024 * 1024, "workspace");

constexpr int LDS_BYTES = 160 * 1024;
#define SC1 16
#define OOB 0x7fffff00u
#define EMPTY 0xffffffffu
#define SPIN_CAP (1u << 20)
#define DELAY_A 9
#define DELAY_B 150

#define QKV_AHEAD 20
#define QKV_URGENT 8
#define SIGPOS 0
#define WOPRE 2
#define PCAUX 16
#define KB_EDGE 8
#define POLL_SLEEP 1
#define KB_VAL DELAY_B
#define SLEEPN(c, r) __builtin_amdgcn_s_sleep(c)
#define g_delay 0
#define PSLOT(l, s) (s)
#define SB() __builtin_amdgcn_sched_barrier(0)
#define MFMA(a, b, c) __builtin_amdgcn_mfma_f32_16x16x32_bf16(a, b, c, 0, 0, 0)
#define BC8(x) __builtin_bit_cast(bf16x8, x)
#define MA0(acc, A, B) asm volatile("v_mfma_f32_16x16x32_bf16 %0, %1, %2, 0" : "=v"(acc) : "a"(A), "v"(B))
#define MA1(acc, A, B) asm volatile("v_mfma_f32_16x16x32_bf16 %0, %1, %2, %0" : "+v"(acc) : "a"(A), "v"(B))
#define MV0(acc, A, B) asm volatile("v_mfma_f32_16x16x32_bf16 %0, %1, %2, 0" : "=v"(acc) : "v"(A), "v"(B))
#define MV1(acc, A, B) asm volatile("v_mfma_f32_16x16x32_bf16 %0, %1, %2, %0" : "+v"(acc) : "v"(A), "v"(B))
#define NOP(n) asm volatile("s_nop %0" :: "i"(n))
#define PINA(x) asm volatile("" : "+a"(x))
#define PINV(x) asm volatile("" : "+v"(x))
#define DPP8(x) ((unsigned)__builtin_amdgcn_update_dpp(0, (int)(x), 0x128, 0xf, 0xf, false))
#define DRAIN() asm volatile("s_waitcnt vmcnt(0)" ::: "memory")
#define MEMFENCE() asm volatile("" ::: "memory")
#define LBAR() do { asm volatile("s_waitcnt lgkmcnt(0)" ::: "memory"); __builtin_amdgcn_s_barrier(); asm volatile("" ::: "memory"); } while (0)
#define LD128(off, so) __builtin_amdgcn_raw_buffer_load_b128(ws, (off), (so), SC1)
#define LD64(off, so) __builtin_amdgcn_raw_buffer_load_b64(ws, (off), (so), SC1)
#define LD32(off, so) __builtin_amdgcn_raw_buffer_load_b32(ws, (off), (so), SC1)
#define ST128(v, off, so) __builtin_amdgcn_raw_buffer_store_b128((v), ws, (off), (so), SC1)
#define ST64(v, off, so) __builtin_amdgcn_raw_buffer_store_b64((v), ws, (off), (so), SC1)
#define ST32(v, off, so) __builtin_amdgcn_raw_buffer_store_b32((v), ws, (off), (so), SC1)
typedef __amdgpu_buffer_rsrc_t rsrc_t;

__device__ __forceinline__ unsigned pk(float a, float b) { f32x2 v = {a, b}; bfx2 r = __builtin_convertvector(v, bfx2); return __builtin_bit_cast(unsigned, r); }
__device__ __forceinline__ float rb(float a) { return __uint_as_float(pk(a, a) << 16); }
__device__ __forceinline__ float lo16(unsigned u) { return __uint_as_float(u << 16); }
__device__ __forceinline__ float hi16(unsigned u) { return __uint_as_float(u & 0xffff0000u); }
__device__ __forceinline__ float red_max(float x) {
  auto r = __builtin_amdgcn_permlane32_swap(__float_as_uint(x), __float_as_uint(x), false, false);
  x = fmaxf(__uint_as_float(r[0]), __uint_as_float(r[1]));
  auto q = __builtin_amdgcn_permlane16_swap(__float_as_uint(x), __float_as_uint(x), false, false);
  return fmaxf(__uint_as_float(q[0]), __uint_as_float(q[1]));
}
__device__ __forceinline__ float red_sum(float x) {
  auto r = __builtin_amdgcn_permlane32_swap(__float_as_uint(x), __float_as_uint(x), false, false);
  x = __uint_as_float(r[0]) + __uint_as_float(r[1]);
  auto q = __builtin_amdgcn_permlane16_swap(__float_as_uint(x), __float_as_uint(x), false, false);
  return __uint_as_float(q[0]) + __uint_as_float(q[1]);
}
__device__ __forceinline__ float wave_sum(float x) {
  x += __uint_as_float((unsigned)__builtin_amdgcn_update_dpp(0, (int)__float_as_uint(x), 0x121, 0xf, 0xf, false));
  x += __uint_as_float((unsigned)__builtin_amdgcn_update_dpp(0, (int)__float_as_uint(x), 0x122, 0xf, 0xf, false));
  x += __uint_as_float((unsigned)__builtin_amdgcn_update_dpp(0, (int)__float_as_uint(x), 0x124, 0xf, 0xf, false));
  x += __uint_as_float(DPP8(__float_as_uint(x)));
  return red_sum(x);
}

__device__ __forceinline__ bool wait_flags(rsrc_t ws, unsigned off, int n, unsigned stride, int lane) {
  unsigned vo = lane < n ? (unsigned)lane * stride : OOB;
  for (unsigned spins = 0; spins < SPIN_CAP; ++spins) {
    MEMFENCE();
    unsigned v = LD32(vo, off);
    unsigned long long miss = __ballot(v == EMPTY);
    if (!miss) return true;
    const int fm = __builtin_ctzll(miss);
    const unsigned v1 = lane == fm ? vo : OOB;
    if (v != EMPTY) vo = OOB;
    for (; spins < SPIN_CAP; ++spins) {
      __builtin_amdgcn_s_sleep(POLL_SLEEP);
      MEMFENCE();
      if (__all(LD32(v1, off) != EMPTY)) break;
      if ((spins & 63u) == 63u) { if (__builtin_amdgcn_readfirstlane(LD32(0u, O_ABT)) == 1u) return false; }
    }
    if (lane == fm) vo = OOB;
  }
  if (lane == 0) ST32(1u, 0u, O_ABT);
  return false;
}

__device__ __forceinline__ void signal_cu(rsrc_t ws, unsigned* cnt, unsigned off, int lane) {
  if (lane == 0) { unsigned old = atomicAdd(cnt, 1u); if ((old & 3u) == 3u) ST32(1u, 0u, off); }
}
struct Params {
  const u16* x; const u16* mem0; u16* out; char* wsp; int role_mask; int layer_mask;
};

__device__ __forceinline__ u32x4 ln_row(const float (&v)[8]) {
  float s = ((v[0] + v[1]) + (v[2] + v[3])) + ((v[4] + v[5]) + (v[6] + v[7]));
  const float mean = wave_sum(s) * (1.0f / 512.0f);
  float q = 0;
  #pragma unroll
  for (int j = 0; j < 8; ++j) { float d = v[j] - mean; q += d * d; }
  const float var = wave_sum(q) * (1.0f / 512.0f);
  const float mb = rb(mean), rden = __builtin_amdgcn_rcpf(rb(__builtin_amdgcn_sqrtf(rb(rb(var) + 1e-5f))));
  float y[8];
  #pragma unroll
  for (int j = 0; j < 8; ++j) y[j] = rb(v[j] - mb) * rden;
  u32x4 o = {pk(y[0], y[1]), pk(y[2], y[3]), pk(y[4], y[5]), pk(y[6], y[7])};
  return o;
}

constexpr int PB1 = 3 * 16 * 8, PB2 = 128, PB3 = 2 * 16 * 4, PB4 = 56;
constexpr int PREP_B = PB1 + PB2 + PB3 + PB4, LN_B = 512, INIT_B = CTRL_BYTES / 16384 + LN_B;
__global__ void __launch_bounds__(256) k_prep(const u16* x, const u16* Wq, const u16* Wk, const u16* Wv, const u16* Wo, const u16* wg, const u16* wu, const u16* wd, char* wsp) {
  __shared__ u32x4 t[2048];
  int b = blockIdx.x; const int tid = threadIdx.x;
  if (b >= PREP_B + INIT_B - LN_B) {
    const int row = (b - (PREP_B + INIT_B - LN_B)) * 4 + (tid >> 6), lane = tid & 63;
    const u32x4 xr = *(const u32x4*)(x + (size_t)row * 512 + lane * 8);
    float v[8] = {lo16(xr.x), hi16(xr.x), lo16(xr.y), hi16(xr.y), lo16(xr.z), hi16(xr.z), lo16(xr.w), hi16(xr.w)};
    *(u32x4*)(wsp + O_XN + (size_t)row * 1024 + lane * 16) = ln_row(v);
    return;
  }
  if (b >= PREP_B) {
    const u32x4 e = {EMPTY, EMPTY, EMPTY, EMPTY};
    #pragma unroll
    for (int k = 0; k < 4; ++k) ((u32x4*)wsp)[(b - PREP_B) * 1024 + k * 256 + tid] = e;
    return;
  }
  const u16* src; int ld, kk, np, nwin = 16; char* dst; int wstride, tstride;
  if (b < PB1) {
    const int m = b >> 7, id = b & 127; kk = id >> 3; np = id & 7;
    src = m == 0 ? Wq : (m == 1 ? Wk : Wv); ld = 4096;
    dst = wsp + O_WQKV + (size_t)((np * 16 * 3 + m) * 32 + kk) * 1024; wstride = 3 * 32768; tstride = 16384;
  } else if (b < PB1 + PB2) {
    kk = b - PB1; np = 0; src = Wo; ld = 512;
    dst = wsp + O_WO + (size_t)((kk >> 2) * 128 + (kk & 3)) * 1024; wstride = 8192; tstride = 4096;
  } else if (b < PB1 + PB2 + PB3) {
    const int id = b - PB1 - PB2; const int m = id >> 6; kk = (id >> 2) & 15; np = id & 3;
    src = m == 0 ? wg : wu; ld = 1792; if (np == 3) nwin = 8;
    dst = wsp + O_WGU + (size_t)((np * 16 * 2 + m) * 32 + kk) * 1024; wstride = 2 * 32768; tstride = 16384;
  } else {
    kk = b - PB1 - PB2 - PB3; np = 0; src = wd; ld = 512;
    dst = wsp + O_WD + (size_t)((kk >> 2) * 128 + (kk & 3)) * 1024; wstride = 8192; tstride = 4096;
  }
  const int g = tid >> 6, n8 = tid & 63;
  if ((n8 >> 2) < nwin) {
    u32x4 r[8];
    #pragma unroll
    for (int j = 0; j < 8; ++j) r[j] = *(const u32x4*)(src + (size_t)(kk * 32 + g * 8 + j) * ld + np * 512 + n8 * 8);
    #pragma unroll
    for (int c = 0; c < 8; ++c) {
      u32x4 o;
      #pragma unroll
      for (int q = 0; q < 4; ++q) {
        const unsigned lo = r[2 * q][c >> 1], hi = r[2 * q + 1][c >> 1];
        o[q] = (c & 1) ? ((lo >> 16) | (hi & 0xffff0000u)) : ((lo & 0xffffu) | (hi << 16));
      }
      t[((n8 >> 2) * 2 + (c >> 2)) * 64 + g * 16 + (n8 & 3) * 4 + (c & 3)] = o;
    }
  }
  __syncthreads();
  #pragma unroll
  for (int k = 0; k < 8; ++k) {
    const int f = k * 4 + g;
    if ((f >> 1) < nwin) *(u32x4*)(dst + (size_t)(f >> 1) * wstride + (f & 1) * tstride + n8 * 16) = t[f * 64 + n8];
  }
}

__device__ __forceinline__ void role_chain(const Params& p, rsrc_t ws, char* lds, const int l, const int h) {
  const int tid = threadIdx.x, lane = tid & 63, w = __builtin_amdgcn_readfirstlane(tid >> 6), i = lane & 15, g = lane >> 4;
  const int vi = g * 8 + (i & 7);
  bf16x8* ldsWo = (bf16x8*)lds;
  bf16x8* memf = (bf16x8*)(lds + 131072);
  f32x4* sc = (f32x4*)(lds + 131072 + 8192);
  bf16x8* of = (bf16x8*)(lds + 131072 + 8192 + 7168);
  bf16x8* wx = (bf16x8*)(lds + 131072 + 8192 + 7168 + 2048) + w * 192 + lane;
  unsigned* cnt = (unsigned*)(lds + 131072 + 8192 + 7168 + 2048 + 12288);
  bf16x8 wf[3][2][16];
  {
    const bf16x8* src = (const bf16x8*)(p.wsp + O_WQKV) + (size_t)((h * 4 + w) * 96) * 64 + lane;
    const bf16x8* so = (const bf16x8*)(p.wsp + O_WO) + (size_t)h * 8192 + tid;
    #pragma unroll
    for (int m = 0; m < 3; ++m) {
      #pragma unroll
      for (int t = 0; t < 2; ++t)
      #pragma unroll
      for (int kk = 0; kk < 16; ++kk) { wf[m][t][kk] = src[((m * 2 + t) * 16 + kk) * 64]; }
      bf16x8 tw[16];
      if (m < 2) {
        #pragma unroll
        for (int k = 0; k < 16; ++k) tw[k] = so[(m * 16 + k) * 256];
      }
      SB();
      if (m == 2) { wx[0] = wf[2][0][14]; wx[64] = wf[2][0][15]; wx[128] = wf[2][1][14]; }
      if (m < 2) {
        #pragma unroll
        for (int t = 0; t < 2; ++t)
        #pragma unroll
        for (int kk = 0; kk < 16; ++kk) PINA(wf[m][t][kk]);
        #pragma unroll
        for (int k = 0; k < 16; ++k) ldsWo[(m * 16 + k) * 256 + tid] = tw[k];
      }
      SB();
    }
  }
  rsrc_t rM0 = __builtin_amdgcn_make_buffer_rsrc((void*)p.mem0, 0, 8192, 0x00020000);
  const int prm = 8 * (i >> 2) + (i & 3);
  const int kOff = w * 1024 + lane * 16;
  const int vOff = w * 2048 + lane * 16;
  const unsigned oK = O_Q + QKV_SZ + h * 8192, oVT = O_Q + 2 * QKV_SZ + h * 8192;
  const unsigned oFH = O_FH + (l * 64 * 32 + h) * 256;
  const unsigned oKM = O_KM + (l * 64 * 32 + h) * 2048, oVMT = O_VMT + (l * 64 * 32 + h) * 2048;
  const unsigned oMEM = O_MEM + l * 64 * 8192, oPC0 = O_PC + l * 32 * 32 * 512;
  u32x4 kx[2], vx[2];
  if (tid == 0) *cnt = 0u;
  if (!wait_flags(ws, oFH + 4, 2, 4, lane)) return;
  #pragma unroll
  for (int t = 0; t < 2; ++t) { kx[t] = LD128(kOff, oK + t * 4096); vx[t] = LD128(vOff + t * 1024, oVT); }
  __syncthreads();
  const int hi = i >> 3;
  const bf16x8* mfp = memf + g * 8 + (i & 7);
  const bf16x8* wop = ldsWo + w * 2048 + lane;
  const f32x4* scx = sc + hi * 32 + vi;
  const f32x2* scm = (const f32x2*)(sc + 64 + vi) + hi;
  f32x4* scw = sc + w * 96 + vi;
  u32x4* ofw = hi ? (u32x4*)(sc) + 384 + lane : (u32x4*)of + w * 32 + vi;
  const bf16x8* ofr_p = mfp + 960;
  const int tid16 = tid * 16;
  const unsigned pcwOff = vi * 16 + hi * 16384;
  const unsigned kmOff = hi ? OOB : (unsigned)((w * 32 + vi) * 16);
  const unsigned vmOff = g < 2 ? (unsigned)(w * 512 + lane * 8) : OOB;
  const int pcrOff = (lane >> 3) * 2048 + (w * 8 + (lane & 7)) * 16;
  const unsigned memOff = lane < 8 ? (unsigned)(w * 128 + lane * 16) : OOB;
  const unsigned flOff = lane < 2 ? (unsigned)(lane * 4 + 4) : OOB;
  const float cexp = 0.08838834764831845f * 1.4426950408889634f;
  int nsleep = 0;
  for (int s = 0; s < 64; ++s) {
    {
      u32x4 x[2];
      if (s == 0) {
        #pragma unroll
        for (int k = 0; k < 2; ++k) { int c = k * 256 + tid; x[k] = __builtin_amdgcn_raw_buffer_load_b128(rM0, ((c & 7) * 512 + (c >> 3) * 8) * 2, 0, 0); }
      } else {
        for (int d_ = 0; d_ < nsleep; ++d_) __builtin_amdgcn_s_sleep(1);
        unsigned spins = 0;
        for (; spins < SPIN_CAP; ++spins) {
          MEMFENCE();
          #pragma unroll
          for (int k = 0; k < 2; ++k) x[k] = LD128(tid16, oMEM + s * 8192 + k * 4096);
          bool ok = true;
          #pragma unroll
          for (int k = 0; k < 2; ++k) ok &= (x[k].x != EMPTY) && (x[k].y != EMPTY) && (x[k].z != EMPTY) && (x[k].w != EMPTY);
          if (__all(ok)) break;
        }
        if (spins >= SPIN_CAP && lane == 0) ST32(1u, 0u, O_ABT);
      }
      #pragma unroll
      for (int k = 0; k < 2; ++k) ((u32x4*)memf)[tid + k * 256] = x[k];
    }
    const unsigned tstamp = ((unsigned)__builtin_amdgcn_s_memrealtime() >> 1) & 255u;
    LBAR();
    f32x4 aq[2], ak[2], av[2];
    bf16x8 wo[16];
    bf16x8 mf[16];
    #pragma unroll
    for (int kk = 0; kk < 16; ++kk) mf[kk] = mfp[kk * 32];
    SB();
#define QK1(acc, m, kk, t) do { if ((kk) == 0) MA0(acc[t], wf[m][t][kk], mf[kk]); else MA1(acc[t], wf[m][t][kk], mf[kk]); SB(); } while (0)
#define V1(kk, t) do { if ((kk) == 0) MV0(av[t], mf[kk], wf[2][t][kk]); else MV1(av[t], mf[kk], wf[2][t][kk]); SB(); if ((t) == 1 && (kk) >= WOPRE) { wo[(kk) - WOPRE] = wop[((kk) - WOPRE) * 64]; SB(); } } while (0)
#define RL(kk)
#define K2(kk) do { QK1(ak, 1, kk, 0); QK1(ak, 1, kk, 1); RL(kk); } while (0)
    #pragma unroll
    for (int kk = 0; kk < 16; ++kk) { QK1(aq, 0, kk, 0); QK1(aq, 0, kk, 1); RL(kk); }
#define KA(kk) QK1(ak, 1, kk, 0)
#define KB(kk) QK1(ak, 1, kk, 1)
    KA(0); KB(0); KA(1); KB(1); KA(2); KB(2); KA(3); KB(3);
    u32x4 qf = {pk(aq[0][0], aq[0][1]), pk(aq[0][2], aq[0][3]), pk(aq[1][0], aq[1][1]), pk(aq[1][2], aq[1][3])}; SB();
    KA(4); KB(4);
    f32x4 s0, s1, s2;
    MV0(s0, kx[0], qf); SB();
    MV0(s1, kx[1], qf); SB();
    KA(5); KB(5); KA(6); KB(6); KA(7); KB(7); KA(8); KB(8);
    scw[0] = s0; SB(); scw[32] = s1; SB();
    KA(9); KB(9); KA(10);
    LBAR();
    f32x4 xa = scx[0], xb = scx[96], xc = scx[192], xd = scx[288];
    SB();
    KB(10); KA(11); KB(11); KA(12); KB(12);
    unsigned nfl = LD32(flOff, oFH + ((s + 1) & 63) * 8192); SB();
    KA(13);
    xa = xa + xb; SB();
    KB(13);
    xc = xc + xd; SB();
    KA(14);
    xa = xa + xc; SB();
    KB(14);
    float mx, tm;
    asm volatile("v_max3_f32 %0, %1, %2, %3\n\tv_max_f32 %0, %0, %4" : "=&v"(mx) : "v"(xa[0]), "v"(xa[1]), "v"(xa[2]), "v"(xa[3])); SB();
    KA(15); KB(15);
    asm volatile("v_max_f32_dpp %0, %0, %0 row_ror:8 row_mask:0xf bank_mask:0xf\n\tv_mov_b32 %1, %0" : "+v"(mx), "=&v"(tm)); SB();
    V1(0, 0); V1(0, 1);
    asm volatile("v_permlane32_swap_b32 %0, %1\n\tv_max_f32 %0, %0, %1\n\tv_mov_b32 %1, %0" : "+v"(mx), "+v"(tm)); SB();
    V1(1, 0); V1(1, 1);
    asm volatile("v_permlane16_swap_b32 %0, %1\n\tv_max_f32 %0, %0, %1" : "+v"(mx), "+v"(tm)); SB();
    float nm = -mx * cexp; PINV(nm); SB();
    V1(2, 0);
    u32x4 kf = {pk(ak[0][0], ak[0][1]), pk(ak[0][2], ak[0][3]), pk(ak[1][0], ak[1][1]), pk(ak[1][2], ak[1][3])}; SB();
    V1(2, 1); V1(3, 0);
    MV0(s2, kf, qf); SB();
    float e0 = __builtin_amdgcn_exp2f(fmaf(xa[0], cexp, nm)), e1 = __builtin_amdgcn_exp2f(fmaf(xa[1], cexp, nm)); PINV(e0); PINV(e1); SB();
    V1(3, 1);
    float e2 = __builtin_amdgcn_exp2f(fmaf(xa[2], cexp, nm)), e3 = __builtin_amdgcn_exp2f(fmaf(xa[3], cexp, nm)); PINV(e2); PINV(e3); SB();
    V1(4, 0);
    scw[64] = s2; SB();
    unsigned p0 = pk(e0, e1), p1 = pk(e2, e3); PINV(p0); PINV(p1); SB();
    V1(4, 1);
    u32x4 pf = {p0, p1, DPP8(p0), DPP8(p1)}; PINV(pf); SB();
    V1(5, 0);
    u32x4 ones;
    asm volatile("v_mov_b32 %0, 0x3f803f80\n\tv_mov_b32 %1, 0x3f803f80\n\tv_mov_b32 %2, 0x3f803f80\n\tv_mov_b32 %3, 0x3f803f80" : "=v"(ones.x), "=v"(ones.y), "=v"(ones.z), "=v"(ones.w)); PINV(ones); SB();
    V1(5, 1);
    LBAR();
    f32x2 ma = scm[0], mb = scm[192], mc = scm[384], md = scm[576];
    wf[2][0][14] = wx[0]; wf[2][1][14] = wx[128]; wf[2][0][15] = wx[64];
    SB();
    f32x4 ao[2], al;
    MV0(ao[0], vx[0], pf); SB();
    MV0(ao[1], vx[1], pf); SB();
    MV0(al, ones, pf); SB();
    V1(6, 0);
    ST128(kf, kmOff, oKM + s * 65536); SB();
    V1(6, 1); V1(7, 0);
    ma = ma + mb; mc = mc + md; SB();
    V1(7, 1);
    ma = ma + mc;
    if (g >= 2) { ma[0] = -INFINITY; ma[1] = -INFINITY; }
    SB();
    V1(8, 0);
    float mm;
    asm volatile("v_max3_f32 %0, %1, %2, %3" : "=&v"(mm) : "v"(ma[0]), "v"(ma[1]), "v"(mx)); SB();
    V1(8, 1); V1(9, 0);
    asm volatile("v_max_f32_dpp %0, %0, %0 row_ror:8 row_mask:0xf bank_mask:0xf\n\tv_mov_b32 %1, %0" : "+v"(mm), "=&v"(tm)); SB();
    V1(9, 1); V1(10, 0);
    asm volatile("v_permlane32_swap_b32 %0, %1\n\tv_max_f32 %0, %0, %1\n\tv_mov_b32 %1, %0" : "+v"(mm), "+v"(tm)); SB();
    V1(10, 1); V1(11, 0);
    asm volatile("v_permlane16_swap_b32 %0, %1\n\tv_max_f32 %0, %0, %1" : "+v"(mm), "+v"(tm)); SB();
    float nm2 = -mm * cexp; PINV(nm2); SB();
    V1(11, 1);
    float e4 = __builtin_amdgcn_exp2f(fmaf(ma[0], cexp, nm2)), e5 = __builtin_amdgcn_exp2f(fmaf(ma[1], cexp, nm2)); PINV(e4); PINV(e5); SB();
    V1(12, 0);
    float fx = __builtin_amdgcn_exp2f(fmaf(mx, cexp, nm2));
    unsigned p2 = pk(e4, e5); PINV(fx); PINV(p2); SB();
    V1(12, 1);
    u32x4 pm4 = {p2, DPP8(p2), 0u, 0u}; PINV(pm4); SB();
    V1(13, 0);
    al[0] *= fx; PINV(al); SB();
    V1(13, 1);
    ao[0] *= fx; PINV(ao[0]); SB();
    V1(14, 0);
    MV1(al, ones, pm4); SB();
    ao[1] *= fx; PINV(ao[1]); SB();
    V1(14, 1); V1(15, 0); V1(15, 1);
    NOP(5); SB();
    u32x4 vm0 = {pk(av[0][0], av[0][1]), pk(av[0][2], av[0][3]), 0u, 0u}; SB();
    u32x4 vm1 = {pk(av[1][0], av[1][1]), pk(av[1][2], av[1][3]), 0u, 0u}; SB();
    MV1(ao[0], vm0, pm4); SB();
    float inv = __builtin_amdgcn_rcpf(al[0]); PINV(inv); SB();
    MV1(ao[1], vm1, pm4); SB();
    #pragma unroll
    for (int k = 16 - WOPRE; k < 16; ++k) wo[k] = wop[k * 64];
    NOP(7 - WOPRE); SB();
    u32x4 ofr = {pk(ao[0][0] * inv, ao[0][1] * inv), pk(ao[0][2] * inv, ao[0][3] * inv), pk(ao[1][0] * inv, ao[1][1] * inv), pk(ao[1][2] * inv, ao[1][3] * inv)};
    *ofw = ofr;
    SB();
    { u32x2 t0 = {vm0.x, vm0.y}, t1 = {vm1.x, vm1.y};
      ST64(t0, vmOff, oVMT + s * 65536);
      ST64(t1, vmOff + 256, oVMT + s * 65536); }
    if (s == 63) break;
    SB();
    LBAR();
    bf16x8 ob[4];
    #pragma unroll
    for (int ks = 0; ks < 4; ++ks) ob[ks] = ofr_p[ks * 32];
    SB();
    const unsigned tag = (s / PCRING) & 1, tag0 = tag | (tstamp << 1);
    const unsigned oPC = oPC0 + (s % PCRING) * 1048576;
    f32x4 accA[2], accB[2];
    #pragma unroll
    for (int pr = 0; pr < 5; ++pr) {
      u32x4 o;
      #pragma unroll
      for (int ks = 0; ks < 4; ++ks) {
        if (pr < 4) { if (ks == 0) MV0(accA[pr & 1], wo[(pr & 1) * 8 + ks], ob[ks]); else MV1(accA[pr & 1], wo[(pr & 1) * 8 + ks], ob[ks]); SB(); }
        if (pr < 4) { if (ks == 0) MV0(accB[pr & 1], wo[(pr & 1) * 8 + 4 + ks], ob[ks]); else MV1(accB[pr & 1], wo[(pr & 1) * 8 + 4 + ks], ob[ks]); SB(); }
        if (pr == 4 && ks == 0) { NOP(7); SB(); }
        if (pr > 0 && (ks > 1 || pr == 4)) {
          #pragma unroll
          for (int e = (pr == 4 ? ks : (ks - 2) * 2); e < (pr == 4 ? ks + 1 : (ks - 2) * 2 + 2); ++e) { float v = hi ? accB[(pr - 1) & 1][e] : accA[(pr - 1) & 1][e]; o[e] = e ? ((__float_as_uint(v) & ~1u) | tag) : ((__float_as_uint(v) & ~0x1ffu) | tag0); }
          SB(); }
        if (pr < 2) { wo[(pr & 1) * 8 + ks] = wop[((pr + 2) * 8 + ks) * 64]; wo[(pr & 1) * 8 + 4 + ks] = wop[((pr + 2) * 8 + 4 + ks) * 64]; SB(); }
      }
      if (pr > 0) { ST128(o, pcwOff, oPC + ((w * 8 + 2 * (pr - 1)) * 32 + h) * 512); SB(); }
    }
    const bool kv_ready = __all(nfl != EMPTY);
    {
      const unsigned ko = kv_ready ? (unsigned)kOff : OOB, vo = kv_ready ? (unsigned)vOff : OOB;
      #pragma unroll
      for (int t = 0; t < 2; ++t) { kx[t] = LD128(ko, oK + (s + 1) * 262144 + t * 4096); vx[t] = LD128(vo, oVT + (s + 1) * 262144 + t * 1024); }
      asm volatile("s_waitcnt vmcnt(8)" ::: "memory");
    }
    signal_cu(ws, cnt, oFH + s * 8192 + 128, lane);
    {
      SLEEPN(DELAY_A, (p.role_mask >> 8) & 255);
      u32x4 x[4];
      unsigned spins = 0;
      for (; spins < SPIN_CAP; ++spins) {
        MEMFENCE();
        #pragma unroll
        for (int k = 0; k < 4; ++k) x[k] = __builtin_amdgcn_raw_buffer_load_b128(ws, pcrOff + k * 512, oPC + h * 16384, PCAUX);
        unsigned an = 1, orr = 0;
        #pragma unroll
        for (int k = 0; k < 4; ++k) { an &= x[k].x & x[k].y & x[k].z & x[k].w; orr |= x[k].x | x[k].y | x[k].z | x[k].w; }
        bool ok = tag ? (an & 1) : !(orr & 1);
        if (__all(ok)) break;
      }
      if (spins >= SPIN_CAP && lane == 0) ST32(1u, 0u, O_ABT);
      DRAIN();
      u32x4 y[4];
      #pragma unroll
      for (int k = 0; k < 4; ++k) { y[k] = x[k]; y[k].x &= ~0x1ffu; }
      f32x4 a = (__builtin_bit_cast(f32x4, y[0]) + __builtin_bit_cast(f32x4, y[1])) + (__builtin_bit_cast(f32x4, y[2]) + __builtin_bit_cast(f32x4, y[3]));
      #pragma unroll
      for (int e = 0; e < 4; ++e) {
        float v = a[e];
        v += __uint_as_float(DPP8(__float_as_uint(v)));
        a[e] = red_sum(v);
      }
      u32x2 o = {pk(a[0], a[1]), pk(a[2], a[3])};
      if (o.x == EMPTY) o.x = 0xfffefffeu;
      if (o.y == EMPTY) o.y = 0xfffefffeu;
      ST64(o, memOff, oMEM + (s + 1) * 8192 + (h >> 1) * 512 + (h & 1) * 8);
      {
        const unsigned now = ((unsigned)__builtin_amdgcn_s_memrealtime() >> 1);
        unsigned age = min(min((now - (x[0].x >> 1)) & 255u, (now - (x[1].x >> 1)) & 255u), min((now - (x[2].x >> 1)) & 255u, (now - (x[3].x >> 1)) & 255u));
        age = min(age, DPP8(age));
        { auto r = __builtin_amdgcn_permlane32_swap(age, age, false, false); age = min(r[0], r[1]); }
        { auto r = __builtin_amdgcn_permlane16_swap(age, age, false, false); age = min(r[0], r[1]); }
        const int rem = (int)KB_VAL - ((l ? s >= 57 : s < 6) ? KB_EDGE : 0) - (int)__builtin_amdgcn_readfirstlane(age);
        nsleep = rem > 0 ? (min(rem, 36) * 3) >> 2 : 0;
      }
      if (!kv_ready) {
        (void)wait_flags(ws, oFH + (s + 1) * 8192 + 4, 2, 4, lane);
        #pragma unroll
        for (int t = 0; t < 2; ++t) { kx[t] = LD128(kOff, oK + (s + 1) * 262144 + t * 4096); vx[t] = LD128(vOff + t * 1024, oVT + (s + 1) * 262144); }
      }
    }
  }
  DRAIN();
  signal_cu(ws, cnt, oFH + 63 * 8192 + 128, lane);
#undef V1
#undef RL
#undef QK1
#undef K2
#undef KA
#undef KB
}

template <bool PERM>
__device__ __forceinline__ void stage_x(char* lx, const u32x4 (&xr)[8], int tid) {
  char* b = lx + (tid & 63) * 528 + (tid >> 6) * 16;
  #pragma unroll
  for (int k = 0; k < 8; ++k) *(u32x4*)(b + (PERM ? (k & 1) * 256 + (k >> 1) * 64 : k * 64)) = xr[k];
}
#define XFRAG(base, kk, rt, tokS) (*(const bf16x8*)((base) + (kk) * 2112 + (rt) * (tokS) * 16))

template <bool PERM>
__device__ __forceinline__ void store_partial(rsrc_t ws, char* tp, const f32x4 (&acc)[8][2], unsigned obase, int w, int lane) {
  const int i = lane & 15, g = lane >> 4;
  char* wp = tp + (PERM ? 8 * (i >> 2) + (i & 3) : i) * 272 + g * 16;
  const char* rp = tp + g * 272 + i * 16;
  const unsigned so = g * 1024 + w * 256 + i * 16;
  #pragma unroll
  for (int rt = 0; rt < 2; ++rt)
  #pragma unroll
  for (int c = 0; c < 4; ++c) {
    u32x4 o = {pk(acc[2 * c][rt][0], acc[2 * c][rt][1]), pk(acc[2 * c][rt][2], acc[2 * c][rt][3]), pk(acc[2 * c + 1][rt][0], acc[2 * c + 1][rt][1]), pk(acc[2 * c + 1][rt][2], acc[2 * c + 1][rt][3])};
    *(u32x4*)(wp + rt * (PERM ? 1088 : 4352) + c * 64) = o;
  }
  u32x4 v[8];
  #pragma unroll
  for (int k = 0; k < 8; ++k) v[k] = *(const u32x4*)(rp + k * 1088);
  #pragma unroll
  for (int k = 0; k < 8; ++k) ST128(v[k], so, obase + k * 4096);
}

template <int MM>
__device__ __forceinline__ void role_qkv(const Params& p, rsrc_t ws, char* lds, const int m, const int h) {
  const int tid = threadIdx.x, lane = tid & 63, w = __builtin_amdgcn_readfirstlane(tid >> 6), i = lane & 15, g = lane >> 4;
  int* dec = (int*)(lds + 2 * 34816);
  bf16x8 wf[2][16], wq[16];
  {
    const bf16x8* src = (const bf16x8*)(p.wsp + O_WQKV) + (size_t)(((h * 4 + w) * 3 + m) * 32) * 64 + lane;
    #pragma unroll
    for (int t = 0; t < 2; ++t)
    #pragma unroll
    for (int kk = 0; kk < 16; ++kk) wf[t][kk] = src[(t * 16 + kk) * 64];
    const bf16x8* sq = (const bf16x8*)(p.wsp + O_WQKV) + (size_t)(((h * 4 + w) * 3) * 32 + (MM - 1) * 16) * 64 + lane;
    #pragma unroll
    for (int kk = 0; kk < 16; ++kk) wq[kk] = sq[kk * 64];
    SB();
    #pragma unroll
    for (int kk = 0; kk < 16; ++kk) { PINA(wq[kk]); PINA(wf[0][kk]); }
    SB();
  }
  const int prm = 8 * (i >> 2) + (i & 3);
  constexpr bool ISV = MM == 2;
  constexpr int tokS = 16;
  const char* xb = lds + g * 528 + i * 16;
  const unsigned stOff = ISV ? (unsigned)(w * 2048 + lane * 16) : (unsigned)(w * 1024 + lane * 16);
  int n0 = (p.layer_mask & 1) ? 0 : 64, n1 = (p.layer_mask & 2) ? 0 : 64;
  const unsigned flv = lane < 32 ? (unsigned)lane * 128u : OOB;
  u32x4 xr[8];
  int cur = -1;
  const bool throttle = (p.role_mask & 1) != 0;
  const unsigned l0 = lane == 0 ? 0u : OOB;
  bool credit = false;
  auto poll = [&](unsigned& f1, unsigned& f0, unsigned& fk) {
    f1 = n1 < 64 ? LD32(flv, O_FXN + (64 + n1) * 4096) : EMPTY;
    f0 = n0 < 64 ? 0u : EMPTY;
    fk = (throttle && !credit && n0 >= QKV_AHEAD && n0 < 64) ? LD32(l0, O_FH + ((n0 - QKV_URGENT) * 32 + h) * 256 + 128) : 0u;
  };
  auto decide = [&](unsigned f1, unsigned f0, unsigned fk) -> int { return __all(f1 != EMPTY) ? 64 + n1 : (__all(f0 != EMPTY && fk != EMPTY) ? n0 : -1); };
  auto pick_blocking = [&]() -> int {
    if (w == 0) {
      int d = -2;
      for (unsigned spins = 0; spins < SPIN_CAP; ++spins) {
        MEMFENCE();
        unsigned f1, f0, fk; poll(f1, f0, fk);
        const int dd = decide(f1, f0, fk);
        if (dd >= 0) { d = dd; break; }
        const unsigned long long m1 = __ballot(f1 == EMPTY);
        const unsigned a1 = (n1 < 64 && lane == (int)__builtin_ctzll(m1)) ? flv : OOB;
        const bool w0 = n0 < 64 && __all(f0 != EMPTY);
        bool ab = false;
        for (; spins < SPIN_CAP; ++spins) {
          __builtin_amdgcn_s_sleep(POLL_SLEEP);
          MEMFENCE();
          if (n0 < 64 && !w0) break;
          const unsigned g1 = n1 < 64 ? LD32(a1, O_FXN + (64 + n1) * 4096) : EMPTY;
          const unsigned gk = w0 ? LD32(l0, O_FH + ((n0 - QKV_URGENT) * 32 + h) * 256 + 128) : EMPTY;
          if (__all(g1 != EMPTY) || __all(gk != EMPTY)) break;
          if ((spins & 63u) == 63u) { if (__builtin_amdgcn_readfirstlane(LD32(0u, O_ABT)) == 1u) { ab = true; break; } }
        }
        if (ab) break;
      }
      if (d == -2 && lane == 0) ST32(1u, 0u, O_ABT);
      if (lane == 0) dec[1] = d;
    }
    LBAR();
    return __builtin_amdgcn_readfirstlane(dec[1]);
  };
  auto issue = [&](int item) {
    #pragma unroll
    for (int k = 0; k < 8; ++k) xr[k] = LD128(tid * 16, O_XN + item * 32768 + k * 4096);
    if (item >= 64) { ++n1; credit = true; } else { ++n0; credit = false; }
  };
  if (n0 >= 64 && n1 >= 64) return;
  unsigned* cnt = (unsigned*)(lds + 2 * 34816 + 64);
  if (tid == 0) *cnt = 0u;
  cur = pick_blocking();
  if (cur < 0) return;
  issue(cur);
  const f32x4 z = {0, 0, 0, 0};
  int buf = 0; unsigned pend = 0;
  for (;;) {
    unsigned f1 = EMPTY, f0 = EMPTY, fk = EMPTY;
    stage_x<true>(lds + buf * 34816, xr, tid);
    if (pend) { DRAIN(); signal_cu(ws, cnt, pend, lane); }
    if (w == 0) poll(f1, f0, fk);
    LBAR();
    const char* xq = xb + buf * 34816;
    bf16x8 xf[4][2];
    #pragma unroll
    for (int kk = 0; kk < 4; ++kk) { xf[kk][0] = XFRAG(xq, kk, 0, tokS); xf[kk][1] = XFRAG(xq, kk, 1, tokS); }
    SB();
    f32x4 acc[2][2], aq[2];
    int nxt = -1, nxtv = -1; unsigned nso = 0, nvo = OOB;
    #pragma unroll
    for (int kk = 0; kk < 16; ++kk) {
      #pragma unroll
      for (int t = 0; t < 2; ++t)
      #pragma unroll
      for (int rt = 0; rt < 2; ++rt) {
        if (ISV) acc[t][rt] = MFMA(xf[kk & 3][rt], wf[t][kk], kk ? acc[t][rt] : z);
        else acc[t][rt] = MFMA(wf[t][kk], xf[kk & 3][rt], kk ? acc[t][rt] : z);
      }
      aq[0] = MFMA(wq[kk], xf[kk & 3][0], kk ? aq[0] : z);
      aq[1] = MFMA(wq[kk], xf[kk & 3][1], kk ? aq[1] : z);
      SB();
      if (kk + 4 < 16) { xf[kk & 3][0] = XFRAG(xq, kk + 4, 0, tokS); xf[kk & 3][1] = XFRAG(xq, kk + 4, 1, tokS); }
      if (kk == 6) {
        if (w == 0) { int d = decide(f1, f0, fk); if (lane == 0) *dec = d; }
        LBAR();
        nxtv = *dec;
      }
      if (kk == 7) { nxt = __builtin_amdgcn_readfirstlane(nxtv); nso = O_XN + (nxt & 127) * 32768; nvo = nxt >= 0 ? (unsigned)tid * 16u : OOB; }
      if (kk >= 8) xr[kk - 8] = LD128(nvo, nso + (kk - 8) * 4096);
      SB();
    }
    if (nxt >= 64) { ++n1; credit = true; } else if (nxt >= 0) { ++n0; credit = false; }
    const int s = cur & 63, l = cur >> 6;
    const unsigned ob = O_Q + m * QKV_SZ + (s * 32 + h) * 8192;
    if (ISV) {
      #pragma unroll
      for (int t = 0; t < 2; ++t) {
        u32x4 o = {pk(acc[t][0][0], acc[t][0][1]), pk(acc[t][0][2], acc[t][0][3]), pk(acc[t][1][0], acc[t][1][1]), pk(acc[t][1][2], acc[t][1][3])};
        ST128(o, stOff + t * 1024, ob);
      }
    } else {
      #pragma unroll
      for (int rt = 0; rt < 2; ++rt) {
        u32x4 o = {pk(acc[0][rt][0], acc[0][rt][1]), pk(acc[0][rt][2], acc[0][rt][3]), pk(acc[1][rt][0], acc[1][rt][1]), pk(acc[1][rt][2], acc[1][rt][3])};
        ST128(o, stOff, ob + rt * 4096);
      }
    }
    #pragma unroll
    for (int rt = 0; rt < 2; ++rt) {
      u32x2 o = {pk(aq[rt][0], aq[rt][1]), pk(aq[rt][2], aq[rt][3])};
      ST64(o, w * 1024 + lane * 16 + (MM - 1) * 8, O_Q + (s * 32 + h) * 8192 + rt * 4096);
    }
    pend = O_FH + ((l * 64 + s) * 32 + h) * 256 + m * 4;
    if (nxt < 0) {
      DRAIN();
      signal_cu(ws, cnt, pend, lane); pend = 0;
      if (n0 >= 64 && n1 >= 64) break;
      nxt = pick_blocking();
      if (nxt < 0) break;
      issue(nxt);
    }
    cur = nxt; buf ^= 1;
  }
}

__device__ __forceinline__ void role_att(const Params& p, rsrc_t ws, char* lds, const int l, const int a, const int par) {
  const int tid = threadIdx.x, lane = tid & 63, w = __builtin_amdgcn_readfirstlane(tid >> 6), i = lane & 15, g = lane >> 4;
  u32x4* ofx = (u32x4*)lds + lane;
  bf16x8* lwo = (bf16x8*)(lds + 32768 + 4 * 8704) + w * 1024 + lane;
  bf16x8 wo[2][8][4];
  #pragma unroll
  for (int hh = 0; hh < 2; ++hh) {
    const bf16x8* src = (const bf16x8*)(p.wsp + O_WO) + (size_t)((2 * a + hh) * 32 + w * 8) * 256 + lane;
    #pragma unroll
    for (int c = 0; c < 8; ++c)
    #pragma unroll
    for (int ks = 0; ks < 4; ++ks) { wo[hh][c][ks] = src[(c * 4 + ks) * 64]; if (hh == 1 && c >= 4) lwo[((c - 4) * 4 + ks) * 64] = wo[hh][c][ks]; }
    SB();
    #pragma unroll
    for (int c = 0; c < 8; ++c)
    #pragma unroll
    for (int ks = 0; ks < 4; ++ks) { if (hh * 32 + c * 4 + ks < 44) PINA(wo[hh][c][ks]); }
    SB();
  }
  const int hh = w >> 1, qh = w & 1, h = 2 * a + hh;
  const int prm = 8 * (i >> 2) + (i & 3);
  const int tq = prm + 4 * qh;
  const float cexp = 0.08838834764831845f * 1.4426950408889634f;
  const f32x4 z = {0, 0, 0, 0};
  const unsigned l16 = lane * 16, kmOff = (g * 8 + (i & 7)) * 16, vmOff = (lane & 31) * 8;
  const unsigned oFH = O_FH + (l * 64 * 32 + h) * 256;
  unsigned* cnt = (unsigned*)(lds + 32768 + 4 * 8704 + 65536);
  if (tid == 0) *cnt = 0u;
  u32x4 qf[4], kf[2][4], vf[8];
  auto issue = [&](int s) {
    const unsigned oq = O_Q + (s * 32 + h) * 8192;
    #pragma unroll
    for (int ks = 0; ks < 4; ++ks) {
      qf[ks] = LD128(l16 + ks * 1024, oq + qh * 4096);
      kf[0][ks] = LD128(l16 + ks * 1024, oq + QKV_SZ);
      kf[1][ks] = LD128(l16 + ks * 1024, oq + QKV_SZ + 4096);
    }
    #pragma unroll
    for (int t8 = 0; t8 < 8; ++t8) vf[t8] = LD128(l16 + (t8 & 3) * 1024, oq + 2 * QKV_SZ + (t8 >> 2) * 4096);
    SB();
  };
  u32x4 kmf[4]; u32x2 vmf[8];
  auto issue_km = [&](int s) {
    const unsigned okm = ((l * 64 + s) * 32 + h) * 2048;
    #pragma unroll
    for (int ks = 0; ks < 4; ++ks) kmf[ks] = LD128(kmOff + ks * 512, O_KM + okm);
    #pragma unroll
    for (int t8 = 0; t8 < 8; ++t8) vmf[t8] = LD64(vmOff + t8 * 256, O_VMT + okm);
    SB();
  };
  const unsigned flv = lane < 2 ? (unsigned)lane * 4u + 4u : (lane == 3 ? 128u : OOB);
  if (!wait_flags(ws, oFH + par * 8192 + 4, 2, 4, lane)) return;
  issue(par);
  bool early = false;
  for (int s = par; s < 64; s += 2) {
    f32x4 s0, s1, s2;
    if (!early) {
      #pragma unroll
      for (int ks = 0; ks < 4; ++ks) {
        s0 = MFMA(BC8(kf[0][ks]), BC8(qf[ks]), ks ? s0 : z);
        s1 = MFMA(BC8(kf[1][ks]), BC8(qf[ks]), ks ? s1 : z);
      }
      SB();
      if (!wait_flags(ws, oFH + s * 8192 + 128, 1, 4, lane)) return;
      issue_km(s);
    } else {
      #pragma unroll
      for (int ks = 0; ks < 4; ++ks) {
        s0 = MFMA(BC8(kf[0][ks]), BC8(qf[ks]), ks ? s0 : z);
        s1 = MFMA(BC8(kf[1][ks]), BC8(qf[ks]), ks ? s1 : z);
      }
      SB();
    }
    float sv[12];
    #pragma unroll
    for (int b = 0; b < 4; ++b) {
      sv[b] = (8 * g + b <= tq) ? s0[b] : -INFINITY;
      sv[4 + b] = (8 * g + 4 + b <= tq) ? s1[b] : -INFINITY;
    }
    float mx = fmaxf(fmaxf(fmaxf(sv[0], sv[1]), fmaxf(sv[2], sv[3])), fmaxf(fmaxf(sv[4], sv[5]), fmaxf(sv[6], sv[7])));
    SB();
    #pragma unroll
    for (int ks = 0; ks < 4; ++ks) s2 = MFMA(BC8(kmf[ks]), BC8(qf[ks]), ks ? s2 : z);
    #pragma unroll
    for (int b = 0; b < 4; ++b) { sv[8 + b] = g < 2 ? s2[b] : -INFINITY; mx = fmaxf(mx, sv[8 + b]); }
    mx = red_max(mx);
    const float nm = -mx * cexp;
    float sum = 0;
    #pragma unroll
    for (int b = 0; b < 12; ++b) { sv[b] = __builtin_amdgcn_exp2f(fmaf(sv[b], cexp, nm)); sum += sv[b]; }
    sum = red_sum(sum);
    const float inv = __builtin_amdgcn_rcpf(sum);
    u32x4 pf = {pk(sv[0] * inv, sv[1] * inv), pk(sv[2] * inv, sv[3] * inv), pk(sv[4] * inv, sv[5] * inv), pk(sv[6] * inv, sv[7] * inv)};
    u32x4 pm = {pk(sv[8] * inv, sv[9] * inv), pk(sv[10] * inv, sv[11] * inv), 0u, 0u};
    f32x4 oo[8];
    #pragma unroll
    for (int t8 = 0; t8 < 8; ++t8) oo[t8] = MFMA(BC8(vf[t8]), BC8(pf), z);
    #pragma unroll
    for (int t8 = 0; t8 < 8; ++t8) { u32x4 v0 = {vmf[t8].x, vmf[t8].y, 0u, 0u}; oo[t8] = MFMA(BC8(v0), BC8(pm), oo[t8]); }
    u32x4* ofw = ofx + ((s >> 1) & 1) * 1024;
    #pragma unroll
    for (int ks = 0; ks < 4; ++ks) {
      u32x4 o = {pk(oo[2 * ks][0], oo[2 * ks][1]), pk(oo[2 * ks][2], oo[2 * ks][3]), pk(oo[2 * ks + 1][0], oo[2 * ks + 1][1]), pk(oo[2 * ks + 1][2], oo[2 * ks + 1][3])};
      ofw[(w * 4 + ks) * 64] = o;
    }
    SB();
    if (early) { DRAIN(); signal_cu(ws, cnt, O_FPA + ((l * 64 + s - 2) * 16 + a) * 128, lane); }
    unsigned nfl = LD32(flv, oFH + ((s + 2) & 63) * 8192);
    LBAR();
    f32x4 acc[8][2];
    bf16x8 b0 = BC8(ofw[0]), b1 = BC8(ofw[256]);
    bool pre = false;
    unsigned pvo = OOB, kvo = OOB, vvo = OOB;
    const unsigned noq = O_Q + (((s + 2) & 63) * 32 + h) * 8192, nokm = ((l * 64 + ((s + 2) & 63)) * 32 + h) * 2048;
    #pragma unroll
    for (int q = 0; q < 8; ++q) {
      const int h2 = q >> 2, ks = q & 3;
      bf16x8 n0, n1;
      if (q < 7) { n0 = BC8(ofw[((((q + 1) >> 2) * 2 + 0) * 4 + ((q + 1) & 3)) * 64]); n1 = BC8(ofw[((((q + 1) >> 2) * 2 + 1) * 4 + ((q + 1) & 3)) * 64]); }
      if (q == 2) {
        pre = s < 62 && __all(lane >= 3 || nfl != EMPTY); early = pre && __all(nfl != EMPTY);
        pvo = pre ? l16 : OOB; kvo = early ? kmOff : OOB; vvo = early ? vmOff : OOB;
      }
      if (q >= 4) {
        #pragma unroll
        for (int c = 4; c < 8; ++c) wo[1][c][ks] = lwo[((c - 4) * 4 + ks) * 64];
      }
      SB();
      #pragma unroll
      for (int c = 0; c < 8; ++c) {
        acc[c][0] = MFMA(wo[h2][c][ks], b0, q ? acc[c][0] : z);
        acc[c][1] = MFMA(wo[h2][c][ks], b1, q ? acc[c][1] : z);
        const int j = (q - 2) * 8 + c;
        if (q >= 2 && j < 32) {
          SB();
          if (j < 4) qf[j] = LD128(pvo + j * 1024, noq + qh * 4096);
          else if (j < 12) kf[(j - 4) >> 2][j & 3] = LD128(pvo + (j & 3) * 1024, noq + QKV_SZ + ((j - 4) >> 2) * 4096);
          else if (j < 20) vf[j - 12] = LD128(pvo + (j & 3) * 1024, noq + 2 * QKV_SZ + ((j - 12) >> 2) * 4096);
          else if (j < 24) kmf[j - 20] = LD128(kvo + (j - 20) * 512, O_KM + nokm);
          else vmf[j - 24] = LD64(vvo + (j - 24) * 256, O_VMT + nokm);
          SB();
        }
      }
      SB();
      if (q < 7) { b0 = n0; b1 = n1; }
    }
    store_partial<true>(ws, lds + 32768 + w * 8704, acc, O_PA + (PSLOT(l, s) * 16 + a) * 32768, w, lane);
    if (!early) {
      DRAIN();
      signal_cu(ws, cnt, O_FPA + ((l * 64 + s) * 16 + a) * 128, lane);
      if (s < 62 && !pre) { if (!wait_flags(ws, oFH + (s + 2) * 8192 + 4, 2, 4, lane)) return; issue(s + 2); }
    }
  }
}

__device__ __forceinline__ void role_ffn(const Params& p, rsrc_t ws, char* lds, const int l, const int f) {
  const int tid = threadIdx.x, lane = tid & 63, w = __builtin_amdgcn_readfirstlane(tid >> 6), i = lane & 15, g = lane >> 4;
  u32x4* hx = (u32x4*)(lds + 34816) + lane;
  bf16x8* lwd = (bf16x8*)(lds + 34816 + 8192) + w * 1024 + lane;
  bf16x8 wg[2][2][16], wd[4][4];
  {
    const bf16x8* src = (const bf16x8*)(p.wsp + O_WGU) + (size_t)((f * 4 + w) * 64) * 64 + lane;
    #pragma unroll
    for (int m = 0; m < 2; ++m)
    #pragma unroll
    for (int t = 0; t < 2; ++t) {
      #pragma unroll
      for (int kk = 0; kk < 16; ++kk) wg[m][t][kk] = src[((m * 2 + t) * 16 + kk) * 64];
      SB();
      #pragma unroll
      for (int kk = 0; kk < 16; ++kk) { if ((m * 2 + t) * 16 + kk < 44) PINA(wg[m][t][kk]); }
      SB();
    }
    const bf16x8* sd = (const bf16x8*)(p.wsp + O_WD) + (size_t)(f * 32 + w * 8) * 256 + lane;
    #pragma unroll
    for (int c = 0; c < 4; ++c)
    #pragma unroll
    for (int ks = 0; ks < 4; ++ks) { wd[c][ks] = sd[(c * 4 + ks) * 64]; lwd[(c * 4 + ks) * 64] = sd[((c + 4) * 4 + ks) * 64]; }
  }
  const f32x4 z = {0, 0, 0, 0};
  const char* xb = lds + g * 528 + i * 16;
  const unsigned flv = lane < 32 ? (unsigned)lane * 128u : OOB;
  unsigned* cnt = (unsigned*)(lds + 34816 + 8192 + 65536 + 4 * 8704);
  if (tid == 0) *cnt = 0u;
  u32x4 xr[8];
  auto issue = [&](int s) {
    #pragma unroll
    for (int k = 0; k < 8; ++k) xr[k] = LD128(tid * 16, O_XN1 + (l * 64 + s) * 32768 + k * 4096);
    SB();
  };
  bool early = false, seen = false;
  float hprev = 0.f;
  for (int s = 0; s < 64; ++s) {
    if (!early) { if (!seen && !wait_flags(ws, O_FXN1 + (l * 64 + s) * 4096, 32, 128, lane)) return; issue(s); }
    stage_x<false>(lds, xr, tid);
    LBAR();
    f32x4 ag[2][2][2];
    auto gelu1 = [&](int rt, int t, int b) -> float {
      const float gv = ag[0][t][rt][b], uv = ag[1][t][rt][b];
      const float y2 = gv * fmaf(gv * gv, -0.044715f * 2.302208198144325f, -2.302208198144325f);
      return gv * __builtin_amdgcn_rcpf(1.0f + __builtin_amdgcn_exp2f(y2)) * uv;
    };
    u32x4 ho;
    bf16x8 xf[4];
    #pragma unroll
    for (int kk = 0; kk < 4; ++kk) xf[kk] = XFRAG(xb, kk, 0, 16);
    SB();
    #pragma unroll
    for (int j = 0; j < 32; ++j) {
      const int rt = j >> 4, kk = j & 15;
      #pragma unroll
      for (int m = 0; m < 2; ++m) {
        #pragma unroll
        for (int t = 0; t < 2; ++t) ag[m][t][rt] = MFMA(wg[m][t][kk], xf[j & 3], kk ? ag[m][t][rt] : z);
        SB();
        if (rt == 1 && kk >= 2 && kk < 10 && m == 0) {
          const int e = kk - 2; float hv = gelu1(0, e >> 2, e & 3);
          if (e & 1) { ho[e >> 1] = pk(hprev, hv); } else hprev = hv;
          SB();
        }
      }
      if (j + 4 < 32) xf[j & 3] = XFRAG(xb, (j + 4) & 15, (j + 4) >> 4, 16);
      if (j == 6 && early) { DRAIN(); signal_cu(ws, cnt, O_FPF + ((l * 64 + s - 1) * 16 + f) * 128, lane); }
      if (j == 27) hx[w * 64] = ho;
      SB();
    }
    bf16x8 wl[4];
    #pragma unroll
    for (int c = 0; c < 4; ++c) wl[c] = lwd[(c * 4) * 64];
    unsigned nfl = LD32(flv, O_FXN1 + (l * 64 + ((s + 1) & 63)) * 4096);
    LBAR();
    f32x4 acc[8][2];
    unsigned nvo = OOB; const unsigned nso = O_XN1 + (l * 64 + ((s + 1) & 63)) * 32768;
    #pragma unroll
    for (int rt = 0; rt < 2; ++rt) {
      bf16x8 hb[4];
      #pragma unroll
      for (int ks = 0; ks < 4; ++ks) hb[ks] = BC8(hx[(rt * 4 + ks) * 64]);
      SB();
      #pragma unroll
      for (int ks = 0; ks < 4; ++ks) {
        bf16x8 wn[4];
        #pragma unroll
        for (int c = 0; c < 4; ++c) wn[c] = lwd[(c * 4 + ((ks + 1) & 3)) * 64];
        SB();
        #pragma unroll
        for (int c = 0; c < 8; ++c) {
          acc[c][rt] = MFMA(c < 4 ? wd[c & 3][ks] : wl[c & 3], hb[ks], ks ? acc[c][rt] : z);
          if (rt == 0 && (c & 3) == 1) {
            SB();
            const int e = ks * 2 + (c >> 2); float hv = gelu1(1, e >> 2, e & 3);
            if (e & 1) { ho[e >> 1] = pk(hprev, hv); } else hprev = hv;
            SB();
          }
          if (rt == 1 && (c & 3) == 1) { SB(); xr[ks * 2 + (c >> 2)] = LD128(nvo, nso + (ks * 2 + (c >> 2)) * 4096); SB(); }
        }
        SB();
        #pragma unroll
        for (int c = 0; c < 4; ++c) wl[c] = wn[c];
      }
      if (rt == 0) {
        hx[(4 + w) * 64] = ho;
        early = s < 63 && __all(nfl != EMPTY);
        nvo = early ? (unsigned)tid * 16u : OOB;
        LBAR();
      }
    }
    store_partial<false>(ws, lds + 34816 + 8192 + 65536 + w * 8704, acc, O_PF + (PSLOT(l, s) * 14 + f) * 32768, w, lane);
    if (!early) {
      const unsigned nf2 = LD32(flv, O_FXN1 + (l * 64 + ((s + 1) & 63)) * 4096);
      DRAIN();
      signal_cu(ws, cnt, O_FPF + ((l * 64 + s) * 16 + f) * 128, lane);
      seen = __all(nf2 != EMPTY);
    }
  }
}

__device__ __forceinline__ void role_red(const Params& p, rsrc_t ws, char* lds, const int r) {
  const int tid = threadIdx.x, lane = tid & 63, w = __builtin_amdgcn_readfirstlane(tid >> 6);
  u32x4* xres = (u32x4*)lds + lane;
  const int st = w & 1, l = w >> 1;
  if (!(p.layer_mask & (1 << l)) || !(p.role_mask & (st ? 16 : 8))) return;
  const int np = st ? 14 : 16;
  for (int s = 0; s < 64; ++s) {
    u32x4 xv;
    if (w == 0) xv = *(const u32x4*)(p.x + ((size_t)(s * 32 + r) * 512 + lane * 8));
    if (!wait_flags(ws, (st ? O_FPF : O_FPA) + (l * 64 + s) * 2048, np, 128, lane)) return;
    const unsigned ob = (st ? O_PF + PSLOT(l, s) * 14 * 32768 : O_PA + PSLOT(l, s) * 16 * 32768) + r * 1024;
    u32x4 a[16];
    #pragma unroll
    for (int k = 0; k < 16; ++k) { if (k < 14 || !st) a[k] = LD128(lane * 16, ob + k * 32768); else a[k] = (u32x4){0u, 0u, 0u, 0u}; }
    if (w > 0) xv = xres[s * 64];
    f32x4 sa = {0, 0, 0, 0}, sb = {0, 0, 0, 0};
    #pragma unroll
    for (int k = 0; k < 16; ++k) {
      sa += (f32x4){lo16(a[k].x), hi16(a[k].x), lo16(a[k].y), hi16(a[k].y)};
      sb += (f32x4){lo16(a[k].z), hi16(a[k].z), lo16(a[k].w), hi16(a[k].w)};
    }
    float v[8];
#define RBI(x) rb(x)
    v[0] = rb(lo16(xv.x) + RBI(sa[0])); v[1] = rb(hi16(xv.x) + RBI(sa[1])); v[2] = rb(lo16(xv.y) + RBI(sa[2])); v[3] = rb(hi16(xv.y) + RBI(sa[3]));
    v[4] = rb(lo16(xv.z) + RBI(sb[0])); v[5] = rb(hi16(xv.z) + RBI(sb[1])); v[6] = rb(lo16(xv.w) + RBI(sb[2])); v[7] = rb(hi16(xv.w) + RBI(sb[3]));
    u32x4 xo = {pk(v[0], v[1]), pk(v[2], v[3]), pk(v[4], v[5]), pk(v[6], v[7])};
    if (w == 3) {
      *(u32x4*)(p.out + ((size_t)(s * 32 + r) * 512 + lane * 8)) = xo;
    } else {
      xres[s * 64] = xo;
      const unsigned od = st ? O_XN + (64 + s) * 32768 : O_XN1 + (l * 64 + s) * 32768;
      ST128(ln_row(v), lane * 16, od + r * 1024);
      asm volatile("s_waitcnt vmcnt(0) lgkmcnt(0)" ::: "memory");
      if (lane == 0) ST32(1u, r * 128, st ? O_FXN + (64 + s) * 4096 : O_FXN1 + (l * 64 + s) * 4096);
    }
  }
}

__global__ void __launch_bounds__(256, 1) k_main(Params p) {
  extern __shared__ __attribute__((aligned(16))) char lds[];
  rsrc_t ws = __builtin_amdgcn_make_buffer_rsrc(p.wsp, 0, 256u * 1024 * 1024, 0x00020000);
#define MAPMODE 0
  int b = blockIdx.x;
  {
    const int x = b & 7, y = b >> 3;
    if (MAPMODE == 1) b = x < 2 ? x * 32 + y : 64 + y * 6 + (x - 2);
    if (MAPMODE == 2) b = y < 16 ? (x < 4 ? (x >> 1) * 32 + (x & 1) * 16 + y : 64 + y * 4 + (x - 4)) : 128 + (y - 16) * 8 + x;
    if (MAPMODE == 3) b = y < 8 ? (x >> 2) * 32 + (x & 3) * 8 + y : b;
  }
  if (b < 64) { if ((p.role_mask & 1) && (p.layer_mask & (1 << (b >> 5)))) role_chain(p, ws, lds, b >> 5, b & 31); }
  else if (b < 96) { if (p.role_mask & 2) role_qkv<2>(p, ws, lds, 2, b & 31); }
  else if (b < 128) { if (p.role_mask & 2) role_qkv<1>(p, ws, lds, 1, b & 31); }
  else if (b < 192) { const int l = (b - 128) >> 5; if ((p.role_mask & 4) && (p.layer_mask & (1 << l))) role_att(p, ws, lds, l, b & 15, (b >> 4) & 1); }
  else if (b < 220) { const int l = (b - 192) >= 14; if ((p.role_mask & 64) && (p.layer_mask & (1 << l))) role_ffn(p, ws, lds, l, b - 192 - 14 * l); }
  else if (b < 252) { if (p.role_mask & (8 | 16 | 32)) role_red(p, ws, lds, b - 220); }
}

extern "C" void kernel_launch(void* const* d_in, const int* in_sizes, int n_in, void* d_out, int out_size, void* d_ws, size_t ws_size, hipStream_t stream) {
  const u16* x = (const u16*)d_in[0]; const u16* mem0 = (const u16*)d_in[1];
  const u16* Wq = (const u16*)d_in[2]; const u16* Wk = (const u16*)d_in[3]; const u16* Wv = (const u16*)d_in[4]; const u16* Wo = (const u16*)d_in[5];
  const u16* wg = (const u16*)d_in[8]; const u16* wu = (const u16*)d_in[9]; const u16* wd = (const u16*)d_in[10];
  (void)hipFuncSetAttribute((const void*)k_main, hipFuncAttributeMaxDynamicSharedMemorySize, LDS_BYTES);
  k_prep<<<PREP_B + INIT_B, 256, 0, stream>>>(x, Wq, Wk, Wv, Wo, wg, wu, wd, (char*)d_ws);
  Params p{x, mem0, (u16*)d_out, (char*)d_ws, 127 | g_delay, 3};
  k_main<<<256, 256, LDS_BYTES, stream>>>(p);
}
```
